```python
import torch

def _softmax(x, axis=-1):
    return torch.softmax(x, dim=axis)
def _gelu(x):
    return torch.nn.functional.gelu(x, approximate="tanh")
def _silu(x):
    return torch.nn.functional.silu(x)
def _sigmoid(x):
    return torch.sigmoid(x)
def _layernorm(x, axis=-1, eps=1e-5):
    m = x.mean(dim=axis, keepdim=True)
    v = x.var(dim=axis, keepdim=True, unbiased=False)
    return (x - m) / torch.sqrt(v + eps)
def _scatter_add(tgt, idx, src, axis=0):
    return tgt.clone().index_add_(axis, idx, src)
def _segment_sum(src, seg, n):
    out = torch.zeros((n,) + src.shape[1:], dtype=src.dtype)
    return out.index_add_(0, seg, src)
def _pad(x, pw):
    flat = tuple(v for pair in reversed(pw) for v in pair)
    return torch.nn.functional.pad(x, flat)

kernel_name = 'recurrent_scan__mha_block_cached__mixer_token_mlp__decode'

def setup_inputs():

    _w = (lambda a: (lambda t: t.to(torch.bfloat16) if t.is_floating_point() else t)(torch.as_tensor(a)))
    def _A_setup():
        B, Sq, D = 8, 1, 512
        H, Sk, Dh = 32, 8192, 128
        return {
            "x": _w((lambda *s: torch.randn(*s, dtype=torch.bfloat16) * 0.4)(B, Sq, D)),
            "k_cache": _w((lambda *s: torch.randn(*s, dtype=torch.bfloat16) * 1.6)(B, H, Sk, Dh)),
            "v_cache": _w((lambda *s: torch.randn(*s, dtype=torch.bfloat16) * 1.6)(B, H, Sk, Dh)),
            "wq": _w((lambda *s: torch.randn(*s, dtype=torch.bfloat16) * 1.6)(D, H * Dh) * (D ** -0.5)),
            "wo": _w((lambda *s: torch.randn(*s, dtype=torch.bfloat16) * 1.6)(H * Dh, D) * ((H * Dh) ** -0.5)),
        }
    _A_in = _A_setup()

    def _B_setup():
        B, S, D, F = 8, 1, 512, 1792
        return {
            "x": _w((lambda *s: torch.randn(*s, dtype=torch.bfloat16) * 0.4)(B, S, D)),
            "w1": _w((lambda *s: torch.randn(*s, dtype=torch.bfloat16) * 1.6)(S, F) * (S ** -0.5)),
            "b1": _w((lambda *s: torch.randn(*s, dtype=torch.bfloat16) * 1.6)(F) * 0.02),
            "w2": _w((lambda *s: torch.randn(*s, dtype=torch.bfloat16) * 1.6)(F, S) * (F ** -0.5)),
            "b2": _w((lambda *s: torch.randn(*s, dtype=torch.bfloat16) * 1.6)(S) * 0.02),
        }
    _B_in = _B_setup()

    return {
        "A_x": _A_in["x"],
        "A_k_cache": _A_in["k_cache"],
        "A_v_cache": _A_in["v_cache"],
        "A_wq": _A_in["wq"],
        "A_wo": _A_in["wo"],
        "B_x": _B_in["x"],
        "B_w1": _B_in["w1"],
        "B_b1": _B_in["b1"],
        "B_w2": _B_in["w2"],
        "B_b2": _B_in["b2"],
    }

def reference(A_x, A_k_cache, A_v_cache, A_wq, A_wo, B_x, B_w1, B_b1, B_w2, B_b2):
    def _A_ref(x, k_cache, v_cache, wq, wo):
        B, Sq, D = x.shape
        H, Dh = 32, 128
        q = (lambda x, *d: x.permute(*d))((x @ wq).reshape(B, Sq, H, Dh), 0, 2, 1, 3)
        scores = torch.einsum("bhqd,bhkd->bhqk", q, k_cache) * (Dh ** -0.5)
        p = _softmax(scores, axis=-1)
        o = torch.einsum("bhqk,bhkd->bhqd", p, v_cache)
        return (lambda x, *d: x.permute(*d))(o, 0, 2, 1, 3).reshape(B, Sq, H * Dh) @ wo

    def _B_ref(x, w1, b1, w2, b2):
        B, S, D = x.shape
        h = _layernorm(x, axis=-1)
        u = torch.einsum("bsd,sf->bdf", h, w1) + b1
        u = _gelu(u)
        y = torch.einsum("bdf,fs->bds", u, w2) + b2
        return (lambda x, *d: x.permute(*d))(y, 0, 2, 1)

    x = A_x
    xn = _layernorm(x, axis=-1)
    s = _A_ref(xn, A_k_cache, A_v_cache, A_wq, A_wo)
    g = _silu(_B_ref(xn, B_w1, B_b1, B_w2, B_b2))
    return x + s * g
```

```cpp
#include <hip/hip_runtime.h>
#include <hip/hip_bf16.h>
#include <stdint.h>

#define GAS __attribute__((address_space(1)))
#define LAS __attribute__((address_space(3)))
typedef unsigned u32;
typedef unsigned short u16;
typedef unsigned long long u64;
typedef u32 v4u __attribute__((ext_vector_type(4)));
typedef u32 v2u __attribute__((ext_vector_type(2)));
typedef float v4f __attribute__((ext_vector_type(4)));
typedef float v2f __attribute__((ext_vector_type(2)));
typedef __bf16 bf16x2 __attribute__((ext_vector_type(2)));

#define RLX_AGENT __ATOMIC_RELAXED, __HIP_MEMORY_SCOPE_AGENT

constexpr int NB = 8, NH = 32, SK = 8192, DH = 128, DM = 512;
constexpr int TILE = 4096, NSLOT = 32, TPH = SK * DH * 2 / TILE;
constexpr int ITERS = TPH / 4;
constexpr int RING_OFF = 0;
constexpr int SC_OFF = RING_OFF + NSLOT * TILE;
constexpr int QP_OFF = SC_OFF + SK * 2;
constexpr int OP_OFF = QP_OFF + 2048;
constexpr int PV_OFF = OP_OFF + 2048;
constexpr int GH_OFF = PV_OFF + 4096;
constexpr int CT_OFF = GH_OFF + 2048;
constexpr int CT_LANDED = CT_OFF;
constexpr int CT_FREE = CT_OFF + 256;
constexpr int CT_FLAG = CT_OFF + 1280;
constexpr int CT_MAXV = CT_FLAG + 64, CT_SUMV = CT_FLAG + 80, CT_TAG = CT_FLAG + 96, CT_WIN = CT_FLAG + 112;
constexpr int CT_BYTES = 1536;
constexpr int LDS_BYTES = CT_OFF + CT_BYTES;
static_assert(LDS_BYTES <= 160 * 1024 && LDS_BYTES > 80 * 1024, "one workgroup per CU");
constexpr size_t WS_INBOX = 0;
constexpr size_t WS_EPOCH = (size_t)NB * 32 * 32 * 128;
constexpr size_t WS_PROG = 4u << 20;
constexpr u32 TAG_STEP = 0x9E3779B9u;
constexpr u32 SPIN_CAP = 1u << 18;
constexpr int WAITN = 19, WMIN = WAITN - 2, WMAX = WAITN + 3;
constexpr int RUNLEN = 16;
constexpr int PRE = 5;
constexpr float CGAIN = 400.f;
constexpr int TAU = 134;
constexpr int PSHIFT = 9, PSTRIDE = 256, CSTOP = 24;
constexpr int AUX_SC0 = 1, AUX_NT = 2;

__device__ __forceinline__ float bf_lo(u32 u) { return __uint_as_float(u << 16); }
__device__ __forceinline__ float bf_hi(u32 u) { return __uint_as_float(u & 0xffff0000u); }
__device__ __forceinline__ u32 pk_bf16(float lo, float hi) {
    bf16x2 v = {(__bf16)lo, (__bf16)hi};
    return __builtin_bit_cast(u32, v);
}
__device__ __forceinline__ float bfr(float v) { return bf_lo(pk_bf16(v, v)); }
__device__ __forceinline__ float dot2(u32 a, u32 b, float acc) {
    return __builtin_amdgcn_fdot2_f32_bf16(__builtin_bit_cast(bf16x2, a), __builtin_bit_cast(bf16x2, b), acc, false);
}
template <int CTRL> __device__ __forceinline__ float dpp(float v) {
    return __builtin_bit_cast(float, __builtin_amdgcn_mov_dpp(__builtin_bit_cast(int, v), CTRL, 0xf, 0xf, true));
}
constexpr int XOR1 = 0xB1, XOR2 = 0x4E, XOR7 = 0x141, XOR8 = 0x128;
__device__ __forceinline__ float fold32(float a, float b) {
    auto s = __builtin_amdgcn_permlane32_swap(__float_as_uint(a), __float_as_uint(b), false, false);
    return __uint_as_float(s[0]) + __uint_as_float(s[1]);
}
__device__ __forceinline__ float fold16(float a, float b) {
    auto s = __builtin_amdgcn_permlane16_swap(__float_as_uint(a), __float_as_uint(b), false, false);
    return __uint_as_float(s[0]) + __uint_as_float(s[1]);
}
__device__ __forceinline__ float wave_sum(float v) {
    v += dpp<XOR1>(v); v += dpp<XOR2>(v); v += dpp<XOR7>(v); v += dpp<XOR8>(v);
    v = fold16(v, v); return fold32(v, v);
}
__device__ __forceinline__ float wave_max(float v) {
    v = fmaxf(v, dpp<XOR1>(v)); v = fmaxf(v, dpp<XOR2>(v)); v = fmaxf(v, dpp<XOR7>(v)); v = fmaxf(v, dpp<XOR8>(v));
    auto s = __builtin_amdgcn_permlane16_swap(__float_as_uint(v), __float_as_uint(v), false, false);
    v = fmaxf(__uint_as_float(s[0]), __uint_as_float(s[1]));
    auto t = __builtin_amdgcn_permlane32_swap(__float_as_uint(v), __float_as_uint(v), false, false);
    return fmaxf(__uint_as_float(t[0]), __uint_as_float(t[1]));
}
__device__ __forceinline__ v2f fold_rows(const float (&a)[8]) {
    const float x0 = fold32(a[0], a[4]), x1 = fold32(a[1], a[5]), x2 = fold32(a[2], a[6]), x3 = fold32(a[3], a[7]);
    v2f o; o.x = fold16(x0, x2); o.y = fold16(x1, x3); return o;
}
__device__ __forceinline__ void ln8(const float (&v)[8], float (&o)[8]) {
    float s = 0.f;
#pragma unroll
    for (int i = 0; i < 8; ++i) s += v[i];
    const float mean = wave_sum(s) * (1.f / DM);
    float q = 0.f;
#pragma unroll
    for (int i = 0; i < 8; ++i) { const float d = v[i] - mean; q = fmaf(d, d, q); }
    const float var = bfr(wave_sum(q) * (1.f / DM));
    const float m = bfr(mean);
    const float den = bfr(__builtin_amdgcn_sqrtf(bfr(var + 1e-5f)));
    const float rc = __builtin_amdgcn_rcpf(den);
#pragma unroll
    for (int i = 0; i < 8; ++i) {
        const float d = bfr(v[i] - m), t = d * rc;
        o[i] = fmaf(fmaf(-t, den, d), rc, t);
    }
}
__device__ __forceinline__ void unpack8(v4u u, float (&v)[8]) {
#pragma unroll
    for (int e = 0; e < 4; ++e) { v[2 * e] = bf_lo(u[e]); v[2 * e + 1] = bf_hi(u[e]); }
}
__device__ __forceinline__ void store16_agent(GAS char* p, v4u v) {
    asm volatile("global_store_dwordx4 %0, %1, off sc1\n\ts_nop 1" :: "v"(p), "v"(v) : "memory");
}
#define LGKM0() asm volatile("s_waitcnt lgkmcnt(0)" ::: "memory")
#define CFENCE() asm volatile("" ::: "memory")
#define PIN16(w, o) asm volatile("" : "+v"(w[o + 0]), "+v"(w[o + 1]), "+v"(w[o + 2]), "+v"(w[o + 3]), "+v"(w[o + 4]), "+v"(w[o + 5]), "+v"(w[o + 6]), "+v"(w[o + 7]), \
                                      "+v"(w[o + 8]), "+v"(w[o + 9]), "+v"(w[o + 10]), "+v"(w[o + 11]), "+v"(w[o + 12]), "+v"(w[o + 13]), "+v"(w[o + 14]), "+v"(w[o + 15]) :: "memory")

__device__ __forceinline__ void pair_rows(v4u (&w)[32]) {
#pragma unroll
    for (int i = 0; i < 16; ++i)
#pragma unroll
        for (int e = 0; e < 4; ++e) {
            const u32 a = w[2 * i][e], b = w[2 * i + 1][e];
            w[2 * i][e] = __builtin_amdgcn_perm(b, a, 0x05040100u);
            w[2 * i + 1][e] = __builtin_amdgcn_perm(b, a, 0x07060302u);
        }
}
__device__ __forceinline__ v2f gemv32(const v4u (&w)[32], const LAS v4u* vec  ) {
    v4u p[4];
#pragma unroll
    for (int j = 0; j < 4; ++j) p[j] = vec[j];
    float a[8];
#pragma unroll
    for (int e = 0; e < 8; ++e) a[e] = 0.f;
#pragma unroll
    for (int i = 0; i < 16; ++i)
#pragma unroll
        for (int e = 0; e < 4; ++e) {
            a[2 * e] = dot2(w[2 * i][e], p[i >> 2][i & 3], a[2 * e]);
            a[2 * e + 1] = dot2(w[2 * i + 1][e], p[i >> 2][i & 3], a[2 * e + 1]);
        }
    return fold_rows(a);
}
__device__ __forceinline__ void cbar(LAS char* L, int which, int ci, u32& budget) {
    LGKM0();
    *(volatile LAS u32*)(L + CT_FLAG + which * 16 + ci * 4) = 1u;
    for (;;) {
        const v4u f = *(volatile LAS v4u*)(L + CT_FLAG + which * 16);
        if (__builtin_amdgcn_readfirstlane(f.x & f.y & f.z & f.w)) break;
        if (budget == 0) break;
        --budget;
    }
    CFENCE();
}
__device__ __forceinline__ void wait_landed(LAS char* L, u32 tile, u32& landed, u32& budget) {
    while (landed <= tile) {
        landed = __builtin_amdgcn_readfirstlane(*(volatile LAS u32*)(L + CT_LANDED));
        if (landed > tile || budget == 0) break;
        --budget;
        __builtin_amdgcn_s_sleep(1);
    }
    CFENCE();
}

constexpr int ABIT[13] = {8, 10, 13, 16, 9, 11, 19, 15, 18, 17, 20, 12, 14};
constexpr u32 abits(u32 v, int first, int n) {
    u32 o = 0;
    for (int i = 0; i < n; ++i) o |= (v >> i & 1) << ABIT[first + i];
    return o;
}
static_assert(abits(0x1fff, 0, 13) == 0x1fff00, "a permutation of the row bits");
template <int FIRST, int N> __device__ __forceinline__ u32 abits_of(u32 v) {
    u32 o = 0;
#pragma unroll
    for (int i = 0; i < N; ++i) o |= (v >> i & 1) << ABIT[FIRST + i];
    return o;
}
constexpr u64 PRE_OFFS = (u64)(abits(1, 4, 9) >> 8) | (u64)(abits(2, 4, 9) >> 8) << 16 | (u64)(abits(3, 4, 9) >> 8) << 32 | (u64)(abits(4, 4, 9) >> 8) << 48;
static_assert(PRE <= 5 && RUNLEN == 16, "prologue offsets, run table");
constexpr int LAGE = (WMAX + 3) / 4;
static_assert(RING_OFF == 0 && NSLOT * TILE == 0x20000 && WMAX + 4 <= 63 && WMAX == WMIN + 5 && 4 * PRE <= 63, "loader loop constants");
__device__ __forceinline__ void stream_tiles(u32 win, const GAS char* src, u32 tab, u32 li, const u32 (&vo)[4], u32 t, u32 end, u32 pub_addr) {
    u32 tmp, n = end - t, pub = t - ((win + 3) >> 2);
    const u32 slot = RING_OFF + (t & (NSLOT - 1)) * TILE;
    const u32 skip = 4 * (win - WMIN);
    const u32 slo = (u32)(size_t)src, shi = (u32)((size_t)src >> 32);
    asm volatile("v_readlane_b32 s44, %[tab], %[li]\n\t"
                 "s_getpc_b64 s[40:41]\n"
                 "1:\n\t"
                 "s_add_u32 s40, s40, %[skip]\n\t"
                 "s_addc_u32 s41, s41, 0\n\t"
                 "s_add_u32 s40, s40, 2f-1b\n\t"
                 "s_addc_u32 s41, s41, 0\n\t"
                 "s_mov_b32 m0, %[slot]\n\t"
                 "s_add_u32 s42, %[slo], s44\n\t"
                 "s_addc_u32 s43, %[shi], 0\n\t"
                 "s_setpc_b64 s[40:41]\n"
                 "2:\n\t"
                 "s_waitcnt vmcnt(%c[w0])\n\t"
                 "s_waitcnt vmcnt(%c[w1])\n\t"
                 "s_waitcnt vmcnt(%c[w2])\n\t"
                 "s_waitcnt vmcnt(%c[w3])\n\t"
                 "s_waitcnt vmcnt(%c[w4])\n\t"
                 "s_waitcnt vmcnt(%c[w5])\n\t"
                 "global_load_lds_dwordx4 %[v0], s[42:43] nt\n\t"
                 "global_load_lds_dwordx4 %[v1], s[42:43] offset:1024 nt\n\t"
                 "global_load_lds_dwordx4 %[v2], s[42:43] offset:2048 nt\n\t"
                 "global_load_lds_dwordx4 %[v3], s[42:43] offset:3072 nt\n\t"
                 "v_max_i32 %[tmp], 0, %[pub]\n\t"
                 "s_add_u32 %[li], %[li], 1\n\t"
                 "ds_write_b32 %[pa], %[tmp]\n\t"
                 "v_add_u32 %[pub], 1, %[pub]\n\t"
                 "v_readlane_b32 s44, %[tab], %[li]\n\t"
                 "s_add_u32 m0, m0, 0x1000\n\t"
                 "s_and_b32 m0, m0, 0x1f000\n\t"
                 "s_add_u32 s42, %[slo], s44\n\t"
                 "s_addc_u32 s43, %[shi], 0\n\t"
                 "s_sub_u32 %[n], %[n], 1\n\t"
                 "s_cmp_eq_u32 %[n], 0\n\t"
                 "s_cbranch_scc1 3f\n\t"
                 "s_setpc_b64 s[40:41]\n"
                 "3:"
                 : [pub] "+v"(pub), [n] "+s"(n), [li] "+s"(li), [tmp] "=&v"(tmp)
                 : [slo] "s"(slo), [shi] "s"(shi), [pa] "v"(pub_addr), [slot] "s"(slot), [skip] "s"(skip), [tab] "v"(tab),
                   [v0] "v"(vo[0]), [v1] "v"(vo[1]), [v2] "v"(vo[2]), [v3] "v"(vo[3]),
                   [w0] "i"(WMIN), [w1] "i"(WMIN + 1), [w2] "i"(WMIN + 2), [w3] "i"(WMIN + 3), [w4] "i"(WMIN + 4), [w5] "i"(WMIN + 5)
                 : "scc", "s40", "s41", "s42", "s43", "s44", "memory");
}
template <int N> __device__ __forceinline__ void publish_tail(u32 pub_addr) {
    asm volatile("s_waitcnt vmcnt(%c2)\n\tds_write_b32 %0, %1" :: "v"(pub_addr), "v"(2 * TPH - N), "i"(4 * N) : "memory");
    if constexpr (N > 0) publish_tail<N - 1>(pub_addr);
}

__global__ void __launch_bounds__(384, 2)
fused_decode_kernel(const char* __restrict__ kc_, const char* __restrict__ wq_, const char* __restrict__ x_, char* ws_,
                    const char* __restrict__ vc_, const char* __restrict__ wo_,
                    const char* __restrict__ w1_, const char* __restrict__ b1_, const char* __restrict__ w2_,
                    const char* __restrict__ b2_, u16* __restrict__ out_) {
    const GAS char* x = (const GAS char*)x_; const GAS char* kc = (const GAS char*)kc_; const GAS char* vc = (const GAS char*)vc_;
    const GAS char* w1 = (const GAS char*)w1_; const GAS char* b1 = (const GAS char*)b1_; const GAS char* w2 = (const GAS char*)w2_;
    const GAS char* b2 = (const GAS char*)b2_; GAS u16* out = (GAS u16*)out_; GAS char* ws = (GAS char*)ws_;
    __shared__ __attribute__((aligned(1024))) char smem[LDS_BYTES];
    LAS char* L = (LAS char*)smem;
    const int lane = threadIdx.x & 63;
    const int wave = __builtin_amdgcn_readfirstlane(threadIdx.x >> 6);
    const int h = blockIdx.x & 31, b = blockIdx.x >> 5;
    const int wg = b * 32 + h;
    u32 budget = SPIN_CAP;

    const GAS char* src = kc + (size_t)wg * (SK * DH * 2) - 3072;
    u32 vo[4];
#pragma unroll
    for (int g = 0; g < 4; ++g)
        vo[g] = (lane & 15) * 16 + abits_of<0, 2>(lane >> 4) + abits(g, 2, 2) + (3 - g) * 1024;
    {
        u64 pk = PRE_OFFS; u32 xo = 0;
        asm volatile("s_cmp_lg_u32 %[wave], 0\n\t"
                     "s_cbranch_scc1 2f\n\t"
                     "s_mov_b32 m0, 0\n\t"
                     "s_setprio 3\n"
                     "1:\n\t"
                     "s_add_u32 s42, %[slo], %[x]\n\t"
                     "s_addc_u32 s43, %[shi], 0\n\t"
                     "s_and_b32 %[x], s44, 0xffff\n\t"
                     "global_load_lds_dwordx4 %[v0], s[42:43] nt\n\t"
                     "global_load_lds_dwordx4 %[v1], s[42:43] offset:1024 nt\n\t"
                     "global_load_lds_dwordx4 %[v2], s[42:43] offset:2048 nt\n\t"
                     "global_load_lds_dwordx4 %[v3], s[42:43] offset:3072 nt\n\t"
                     "s_add_u32 m0, m0, 0x1000\n\t"
                     "s_lshl_b32 %[x], %[x], 8\n\t"
                     "s_lshr_b64 s[44:45], s[44:45], 16\n\t"
                     "s_cmp_lg_u32 m0, %c[end]\n\t"
                     "s_cbranch_scc1 1b\n"
                     "2:"
                     : [x] "+s"(xo), "+{s[44:45]}"(pk) : [slo] "s"((u32)(size_t)src), [shi] "s"((u32)((size_t)src >> 32)), [wave] "s"(wave), [end] "i"(PRE * TILE),
                       [v0] "v"(vo[0]), [v1] "v"(vo[1]), [v2] "v"(vo[2]), [v3] "v"(vo[3]), "s"(wq_), "s"(x_), "s"(ws_) : "scc", "s42", "s43", "memory");
    }

    if (wave == 0) {
        const u32 lbase = (u32)(size_t)L;
        asm volatile("s_barrier" ::: "memory");
        const u32 pub_addr = lbase + CT_LANDED + lane * 4;
        const u32 free_addr = lbase + CT_FREE;
        const u32 tlo = abits_of<4, 4>(lane), thi = abits_of<8, 5>(lane);
        u32 t = PRE;
        for (;;) {
            v2u f0, f1; u32 wv;
            asm volatile("ds_read2st64_b32 %0, %3 offset0:0 offset1:1\n\tds_read2st64_b32 %1, %3 offset0:2 offset1:3\n\t"
                         "ds_read_b32 %2, %3 offset:%c4\n\ts_waitcnt lgkmcnt(0)"
                         : "=&v"(f0), "=&v"(f1), "=&v"(wv) : "v"(free_addr), "i"(CT_WIN - CT_FREE) : "memory");
            u32 limit = 4 * __builtin_amdgcn_readfirstlane(min(min(f0.x, f0.y), min(f1.x, f1.y))) + NSLOT;
            const u32 win = __builtin_amdgcn_readfirstlane(min(max(wv, (u32)WMIN), (u32)WMAX));
            if (t >= limit) {
                if (budget != 0) { --budget; __builtin_amdgcn_s_sleep(1); continue; }
                limit = ~0u;
            }
            const u32 end = min(limit, (t | (RUNLEN - 1)) + 1);
            stream_tiles(win, src + (u32)__builtin_amdgcn_readlane(thi, (t >> 4) & 31), tlo, t & 15, vo, t, end, pub_addr);
            t = end;
            if (t == TPH) src = vc + (size_t)wg * (SK * DH * 2) - 3072;
            if (t == 2 * TPH) break;
        }
        publish_tail<LAGE - 1>(pub_addr);
        LGKM0();
    } else if (wave == 5) {
        {
            const v4u z = {0u, 0u, 0u, 0u};
            volatile LAS v4u* cw = (volatile LAS v4u*)(L + CT_OFF);
            cw[lane] = z;
            if (lane < CT_BYTES / 16 - 64) cw[64 + lane] = z;
            *(volatile LAS u32*)(L + CT_WIN) = WAITN;
        }
        LGKM0();
        asm volatile("s_barrier" ::: "memory");
        __builtin_amdgcn_sched_barrier(0);
        const v4u xr = *(const GAS v4u*)(x + b * (DM * 2) + lane * 16);
        const u32 xo = *(const GAS u16*)(x + (b * DM + 16 * h + (lane & 15)) * 2);
        const u32 b2r = *(const GAS u16*)b2;
        const u32 tag0 = __hip_atomic_load((GAS u32*)(ws + WS_EPOCH), RLX_AGENT) + TAG_STEP;
        v2u w1r[7], b1r[7], w2r[7];
#pragma unroll
        for (int k = 0; k < 7; ++k) {
            w1r[k] = *(const GAS v2u*)(w1 + lane * 56 + k * 8);
            b1r[k] = *(const GAS v2u*)(b1 + lane * 56 + k * 8);
            w2r[k] = *(const GAS v2u*)(w2 + lane * 56 + k * 8);
        }
        {
            float xv[8], xn[8], hv[8];
            unpack8(xr, xv);
            ln8(xv, xn);
#pragma unroll
            for (int i = 0; i < 8; ++i) xn[i] = bfr(xn[i]);
            ln8(xn, hv);
#pragma unroll
            for (int i = 0; i < 8; ++i) hv[i] = bfr(hv[i]);
            v4f a = {hv[0], hv[1], hv[2], hv[3]}, bb = {hv[4], hv[5], hv[6], hv[7]};
            *(LAS v4f*)(L + GH_OFF + lane * 32) = a;
            *(LAS v4f*)(L + GH_OFF + lane * 32 + 16) = bb;
        }
        float gate = 0.f;
#pragma unroll 1
        for (int d = 0; d < 16; ++d) {
            const float hd = *(const LAS float*)(L + GH_OFF + (16 * h + d) * 4);
            float y = 0.f;
#pragma unroll
            for (int k = 0; k < 14; ++k) {
                const u32 ww = w1r[k >> 1][k & 1], bb = b1r[k >> 1][k & 1], w2 = w2r[k >> 1][k & 1];
                const u32 t = pk_bf16(hd * bf_lo(ww), hd * bf_hi(ww));
                const u32 uu = pk_bf16(bf_lo(t) + bf_lo(bb), bf_hi(t) + bf_hi(bb));
                const float u0 = bf_lo(uu), u1 = bf_hi(uu);
                constexpr float C0 = -2.f * 0.7978845608028654f * 1.4426950408889634f, C1 = C0 * 0.044715f;
                const float z0 = u0 * fmaf(u0 * u0, C1, C0), z1 = u1 * fmaf(u1 * u1, C1, C0);
                const float g0 = u0 * __builtin_amdgcn_rcpf(1.f + __builtin_amdgcn_exp2f(z0));
                const float g1 = u1 * __builtin_amdgcn_rcpf(1.f + __builtin_amdgcn_exp2f(z1));
                y = dot2(pk_bf16(g0, g1), w2, y);
            }
            y = bfr(bfr(wave_sum(y)) + bf_lo(b2r));
            const float gd = bfr(y / (1.f + __expf(-y)));
            if ((lane & 15) == d) gate = gd;
        }
        {
            u32 ref = 0, have = 0, per = (u32)__builtin_amdgcn_s_memrealtime() >> PSHIFT;
            for (;;) {
                const u32 ld = __builtin_amdgcn_readfirstlane(*(volatile LAS u32*)(L + CT_LANDED));
                if (ld >= 2 * TPH - CSTOP || budget == 0) break;
                --budget;
                const u32 now = (u32)__builtin_amdgcn_s_memrealtime();
                const u32 lag = now * 10u - ld * TAU;
                if ((now >> PSHIFT) != per) {
                    per = now >> PSHIFT;
                    GAS char* pw = ws + WS_PROG + (per & 63) * (8 * PSTRIDE), *pr = ws + WS_PROG + ((per - 1) & 63) * (8 * PSTRIDE);
                    if (b == 0 && lane == 0) __hip_atomic_store((GAS u64*)(pw + (h & 7) * PSTRIDE + (h >> 3) * 8), ((u64)(tag0 + per) << 32) | lag, RLX_AGENT);
                    const u64 e = __hip_atomic_load((GAS u64*)(pr + (lane & 7) * PSTRIDE + ((lane >> 3) & 3) * 8), RLX_AGENT);
                    const bool ok = (u32)(e >> 32) == tag0 + per - 1;
                    const int n = __builtin_popcountll(__ballot(ok));
                    const float d = wave_sum(ok ? (float)(int)((u32)e - lag) : 0.f);
                    if (n >= 48) { ref = lag + __builtin_amdgcn_readfirstlane((int)(d / (float)n)); have = 1; }
                }
                if (have) {
                    const float dev = (float)(int)(lag - ref);
                    const float wf = fminf(fmaxf((float)WAITN + __builtin_rintf(dev * (1.f / CGAIN)), (float)WMIN), (float)WMAX);
                    *(volatile LAS u32*)(L + CT_WIN) = (u32)wf;
                }
                __builtin_amdgcn_s_sleep(32);
            }
        }
        for (;;) {
            const v4u f = *(volatile LAS v4u*)(L + CT_FLAG + 48);
            if (__builtin_amdgcn_readfirstlane(f.x & f.y & f.z & f.w)) break;
            if (budget == 0) break;
            --budget;
            __builtin_amdgcn_s_sleep(2);
        }
        __builtin_amdgcn_s_sleep(28);
        CFENCE();
        __builtin_amdgcn_s_setprio(3);
        const u32 tag = __builtin_amdgcn_readfirstlane(*(volatile LAS u32*)(L + CT_TAG));
        GAS u64* in = (GAS u64*)(ws + WS_INBOX + (size_t)wg * 4096) + lane;
        u64 v[8];
#pragma unroll
        for (int k = 0; k < 8; ++k) v[k] = 0;
        u32 pend = 0xffu;
        for (;;) {
#pragma unroll
            for (int k = 0; k < 8; ++k)
                if (pend & (1u << k)) v[k] = __hip_atomic_load(in + k * 64, RLX_AGENT);
#pragma unroll
            for (int k = 0; k < 8; ++k)
                if (__all((u32)(v[k] >> 32) == tag)) pend &= ~(1u << k);
            if (pend == 0 || budget == 0) break;
            --budget;
        }
        float s = 0.f;
#pragma unroll
        for (int k = 0; k < 8; ++k) s += __uint_as_float((u32)v[k]);
        s = fold16(s, s);
        s = fold32(s, s);
        const float res = bf_lo(xo) + bfr(bfr(s) * gate);
        if (lane < 16) out[b * DM + 16 * h + lane] = (u16)pk_bf16(res, res);
        if (h == 0 && lane == 0) __hip_atomic_store((GAS u32*)(ws + WS_EPOCH + b * 128), tag, RLX_AGENT);
    } else {
        __builtin_amdgcn_s_setprio(2);
        const int ci = wave - 1, r = lane >> 4, c = lane & 15;
        v4u xr = *(const GAS v4u*)(x + b * (DM * 2) + lane * 16);
        u32 tag = __hip_atomic_load((GAS u32*)(ws + WS_EPOCH + b * 128), RLX_AGENT);
        v4u w[32];
        const int rot = b;
        {
            const __amdgpu_buffer_rsrc_t rs = __builtin_amdgcn_make_buffer_rsrc((void*)wq_, 0, DM * NH * DH * 2, 0x00020000);
            const u32 vo = 32 * r * (NH * DH * 2) + c * 16, so = 128 * ci * (NH * DH * 2) + h * (DH * 2);
#pragma unroll
            for (int i = 0; i < 32; ++i)
                w[i] = __builtin_amdgcn_raw_buffer_load_b128(rs, vo, so + ((i + 4 * rot) & 31) * (NH * DH * 2), AUX_NT);
        }
        __builtin_amdgcn_sched_barrier(0);
        asm volatile("s_barrier" ::: "memory");
        asm volatile("" : "+v"(xr), "+v"(tag) :: "memory");
        tag += TAG_STEP;
        LAS char* priv = L + PV_OFF + ci * 1024;
        {
            float xv[8], xn[8];
            unpack8(xr, xv);
            ln8(xv, xn);
            v4u o;
#pragma unroll
            for (int e = 0; e < 4; ++e) o[e] = pk_bf16(xn[2 * e], xn[2 * e + 1]);
            v2u o0 = {o.x, o.y}, o1 = {o.z, o.w};
            *(LAS v2u*)(priv + (lane >> 2) * 64 + ((4 * (lane & 3) - 2 * rot) & 15) * 4) = o0;
            *(LAS v2u*)(priv + (lane >> 2) * 64 + ((4 * (lane & 3) + 2 - 2 * rot) & 15) * 4) = o1;
        }
        if (ci == 0) *(volatile LAS u32*)(L + CT_TAG) = tag;
        pair_rows(w);
        {
            const v2f qp = gemv32(w, (const LAS v4u*)(priv + (128 * ci + 32 * r) * 2));
            *(LAS v2f*)(L + QP_OFF + ci * 512 + (8 * c + 2 * r) * 4) = qp;
        }
        CFENCE();
        cbar(L, 0, ci, budget);
        u32 q[4];
        {
            v4f s0 = *(const LAS v4f*)(L + QP_OFF + c * 32), s1 = *(const LAS v4f*)(L + QP_OFF + c * 32 + 16);
#pragma unroll
            for (int k = 1; k < 4; ++k) { s0 += *(const LAS v4f*)(L + QP_OFF + k * 512 + c * 32); s1 += *(const LAS v4f*)(L + QP_OFF + k * 512 + c * 32 + 16); }
            q[0] = pk_bf16(s0.x, s0.y); q[1] = pk_bf16(s0.z, s0.w); q[2] = pk_bf16(s1.x, s1.y); q[3] = pk_bf16(s1.z, s1.w);
        }
        tag = *(volatile LAS u32*)(L + CT_TAG);
        volatile LAS u32* freew = (volatile LAS u32*)(L + CT_FREE + ci * 256 + lane * 4);
        LAS char* myscores = L + SC_OFF + ci * 4096;
        u32 landed = 0;
        float mx = -INFINITY;
        const bool lo8 = c < 8, lo4 = (c & 4) == 0;
        const __amdgpu_buffer_rsrc_t wors = __builtin_amdgcn_make_buffer_rsrc((void*)wo_, 0, DM * NH * DH * 2, 0x00020000);
        const u32 wovo = (h * DH + 32 * r) * (DM * 2) + (128 * ci + 8 * c) * 2;
#pragma unroll
        for (int seg = 0; seg < 8; ++seg) {
#pragma unroll
            for (int i = 4 * seg; i < 4 * seg + 4; ++i) {
                const u32 vo = wovo + (i & 3) * (DM * 2), so = ((seg + rot) & 7) * (4 * DM * 2);
                if (seg) w[i] = __builtin_amdgcn_raw_buffer_load_b128(wors, vo, so, 0);
                else w[i] = __builtin_amdgcn_raw_buffer_load_b128(wors, vo, so, AUX_NT);
            }
            if (seg == 0) {
                __builtin_amdgcn_sched_barrier(0);
#pragma unroll
                for (int i = 0; i < 4; ++i) w[28 + i] = __builtin_amdgcn_raw_buffer_load_b128(wors, wovo + i * (DM * 2), rot * (4 * DM * 2), AUX_SC0);
            }
            if (seg == 1) asm volatile("" :: "v"(w[28]), "v"(w[29]), "v"(w[30]), "v"(w[31]));
            __builtin_amdgcn_sched_barrier(0);
#pragma unroll 1
            for (int it = seg * (ITERS / 8); it < (seg + 1) * (ITERS / 8); ++it) {
                const u32 tile = 4 * it + ci;
                wait_landed(L, tile, landed, budget);
                const LAS v4u* tp = (const LAS v4u*)(L + RING_OFF + (tile & (NSLOT - 1)) * TILE + lane * 16);
                v4u k0 = tp[0], k1 = tp[64], k2 = tp[128], k3 = tp[192];
                LGKM0();
                *freew = it + 1;
                asm volatile("" : "+v"(k0), "+v"(k1), "+v"(k2), "+v"(k3) :: "memory");
                float a0 = 0.f, a1 = 0.f, a2 = 0.f, a3 = 0.f;
#pragma unroll
                for (int e = 0; e < 4; ++e) { a0 = dot2(k0[e], q[e], a0); a1 = dot2(k1[e], q[e], a1); a2 = dot2(k2[e], q[e], a2); a3 = dot2(k3[e], q[e], a3); }
                const float b0 = (lo8 ? a0 : a2) + dpp<XOR8>(lo8 ? a2 : a0);
                const float b1 = (lo8 ? a1 : a3) + dpp<XOR8>(lo8 ? a3 : a1);
                float s = (lo4 ? b0 : b1) + dpp<XOR7>(lo4 ? b1 : b0);
                s += dpp<XOR2>(s);
                s += dpp<XOR1>(s);
                const float sc = bfr(s) * 0.088388347648318440f;
                const u32 sb = pk_bf16(sc, sc);
                mx = fmaxf(mx, bf_lo(sb));
                *(LAS u16*)(myscores + it * 32 + r * 8 + (c >> 2) * 2) = (u16)sb;
            }
        }
        pair_rows(w);
        PIN16(w, 0); PIN16(w, 16);
        mx = wave_max(mx);
        *(volatile LAS float*)(L + CT_MAXV + ci * 4) = mx;
        cbar(L, 1, ci, budget);
        {
            const v4f mv = *(volatile LAS v4f*)(L + CT_MAXV);
            const float gm = fmaxf(fmaxf(mv.x, mv.y), fmaxf(mv.z, mv.w));
            LAS v4u* sp = (LAS v4u*)(myscores + lane * 16);
            float ex[32];
            float sum = 0.f;
#pragma unroll
            for (int j = 0; j < 4; ++j) {
                const v4u u = sp[j * 64];
#pragma unroll
                for (int e = 0; e < 4; ++e) {
                    const float e0 = __builtin_amdgcn_exp2f((bf_lo(u[e]) - gm) * 1.4426950408889634f);
                    const float e1 = __builtin_amdgcn_exp2f((bf_hi(u[e]) - gm) * 1.4426950408889634f);
                    ex[8 * j + 2 * e] = e0; ex[8 * j + 2 * e + 1] = e1;
                    sum += e0 + e1;
                }
            }
            sum = wave_sum(sum);
            *(volatile LAS float*)(L + CT_SUMV + ci * 4) = sum;
            cbar(L, 2, ci, budget);
            const v4f sv = *(volatile LAS v4f*)(L + CT_SUMV);
            const float inv = 1.f / ((sv.x + sv.y) + (sv.z + sv.w));
#pragma unroll
            for (int j = 0; j < 4; ++j) {
                v4u u;
#pragma unroll
                for (int e = 0; e < 4; ++e) u[e] = pk_bf16(ex[8 * j + 2 * e] * inv, ex[8 * j + 2 * e + 1] * inv);
                sp[j * 64] = u;
            }
        }
        CFENCE();
        float acc[8];
#pragma unroll
        for (int e = 0; e < 8; ++e) acc[e] = 0.f;
#pragma unroll 1
        for (int it = 0; it < ITERS; ++it) {
            const u32 tile = TPH + 4 * it + ci;
            const v2u pp = *(const LAS v2u*)(myscores + it * 32 + r * 8);
            wait_landed(L, tile, landed, budget);
            const LAS v4u* tp = (const LAS v4u*)(L + RING_OFF + (tile & (NSLOT - 1)) * TILE + lane * 16);
            v4u v0 = tp[0], v1 = tp[64], v2 = tp[128], v3 = tp[192];
            LGKM0();
            *freew = ITERS + it + 1;
            asm volatile("" : "+v"(v0), "+v"(v1), "+v"(v2), "+v"(v3) :: "memory");
#pragma unroll
            for (int e = 0; e < 4; ++e) {
                acc[2 * e] = dot2(__builtin_amdgcn_perm(v1[e], v0[e], 0x05040100u), pp.x, acc[2 * e]);
                acc[2 * e + 1] = dot2(__builtin_amdgcn_perm(v1[e], v0[e], 0x07060302u), pp.x, acc[2 * e + 1]);
            }
#pragma unroll
            for (int e = 0; e < 4; ++e) {
                acc[2 * e] = dot2(__builtin_amdgcn_perm(v3[e], v2[e], 0x05040100u), pp.y, acc[2 * e]);
                acc[2 * e + 1] = dot2(__builtin_amdgcn_perm(v3[e], v2[e], 0x07060302u), pp.y, acc[2 * e + 1]);
            }
        }
        *(LAS v2f*)(L + OP_OFF + ci * 512 + (8 * c + 2 * r) * 4) = fold_rows(acc);
        cbar(L, 3, ci, budget);
        {
            const v2f o0 = *(const LAS v2f*)(L + OP_OFF + lane * 8), o1 = *(const LAS v2f*)(L + OP_OFF + 512 + lane * 8);
            const v2f o2 = *(const LAS v2f*)(L + OP_OFF + 1024 + lane * 8), o3 = *(const LAS v2f*)(L + OP_OFF + 1536 + lane * 8);
            const v2f o = (o0 + o1) + (o2 + o3);
            *(LAS u32*)(priv + r * 64 + ((c - 2 * rot) & 15) * 4) = pk_bf16(o.x, o.y);
        }
        const v2f sp = gemv32(w, (const LAS v4u*)(priv + r * 64));
        const int slot = (h & 7) * 4 + (h >> 3);
        GAS char* g = ws + WS_INBOX + ((size_t)((b * 32 + 8 * ci + (c >> 1)) * 32 + slot)) * 128 + (8 * (c & 1) + 2 * r) * 8;
        store16_agent(g, (v4u){__float_as_uint(sp.x), tag, __float_as_uint(sp.y), tag});
    }
}

extern "C" void kernel_launch(void* const* d_in, const int* in_sizes, int n_in,
                              void* d_out, int out_size, void* d_ws, size_t ws_size,
                              hipStream_t stream) {
    (void)in_sizes; (void)n_in; (void)out_size; (void)ws_size;
    fused_decode_kernel<<<NB * NH, 384, 0, stream>>>(
        (const char*)d_in[1], (const char*)d_in[3], (const char*)d_in[0], (char*)d_ws, (const char*)d_in[2], (const char*)d_in[4],
        (const char*)d_in[6], (const char*)d_in[7], (const char*)d_in[8], (const char*)d_in[9], (u16*)d_out);
}
```

```cpp
#include <hip/hip_runtime.h>
#include <hip/hip_bf16.h>
#include <stdint.h>
#include <stdio.h>

#define GAS __attribute__((address_space(1)))
#define LAS __attribute__((address_space(3)))
typedef unsigned u32;
typedef unsigned short u16;
typedef unsigned long long u64;
typedef u32 v4u __attribute__((ext_vector_type(4)));
typedef u32 v2u __attribute__((ext_vector_type(2)));
typedef float v4f __attribute__((ext_vector_type(4)));
typedef float v2f __attribute__((ext_vector_type(2)));
typedef __bf16 bf16x2 __attribute__((ext_vector_type(2)));
typedef __bf16 bf16x8 __attribute__((ext_vector_type(8)));

#define RLX_AGENT __ATOMIC_RELAXED, __HIP_MEMORY_SCOPE_AGENT

constexpr int NB = 8, NH = 32, SK = 8192, DH = 128, DM = 512;
constexpr int TILE = 4096, NSLOT = 32, TPH = SK * DH * 2 / TILE;
constexpr int ITERS = TPH / 4;
constexpr int RING_OFF = 0;
constexpr int SC_OFF = RING_OFF + NSLOT * TILE;
constexpr int QP_OFF = SC_OFF + SK * 2;
constexpr int OP_OFF = QP_OFF + 2048;
constexpr int PV_OFF = OP_OFF + 2048;
constexpr int GH_OFF = PV_OFF + 4096;
constexpr int CT_OFF = GH_OFF + 2048;
constexpr int CT_LANDED = CT_OFF;
constexpr int CT_FREE = CT_OFF + 256;
constexpr int CT_FLAG = CT_OFF + 1280;
constexpr int CT_MAXV = CT_FLAG + 64, CT_SUMV = CT_FLAG + 80, CT_TAG = CT_FLAG + 96, CT_WIN = CT_FLAG + 112;
constexpr int CT_BYTES = 1536;
constexpr int LDS_BYTES = CT_OFF + CT_BYTES;
static_assert(LDS_BYTES <= 160 * 1024 && LDS_BYTES > 80 * 1024, "one workgroup per CU");
constexpr size_t WS_INBOX = 0;
constexpr size_t WS_EPOCH = (size_t)NB * 32 * 32 * 128;
constexpr size_t WS_PROG = 4u << 20;
constexpr u32 TAG_STEP = 0x9E3779B9u;
constexpr u32 SPIN_CAP = 1u << 18;
constexpr int WAITN = 19, WMIN = WAITN - 2, WMAX = WAITN + 3;
constexpr int RUNLEN = 16;
constexpr int PRE = 5;
constexpr float CGAIN = 400.f;
constexpr int TAU = 134;
constexpr int PSHIFT = 9, PSTRIDE = 256, CSTOP = 24;
constexpr int AUX_SC0 = 1, AUX_NT = 2;

__device__ __forceinline__ float bf_lo(u32 u) { return __uint_as_float(u << 16); }
__device__ __forceinline__ float bf_hi(u32 u) { return __uint_as_float(u & 0xffff0000u); }
__device__ __forceinline__ u32 pk_bf16(float lo, float hi) {
    bf16x2 v = {(__bf16)lo, (__bf16)hi};
    return __builtin_bit_cast(u32, v);
}
__device__ __forceinline__ float bfr(float v) { return bf_lo(pk_bf16(v, v)); }
__device__ __forceinline__ float dot2(u32 a, u32 b, float acc) {
    return __builtin_amdgcn_fdot2_f32_bf16(__builtin_bit_cast(bf16x2, a), __builtin_bit_cast(bf16x2, b), acc, false);
}
template <int CTRL> __device__ __forceinline__ float dpp(float v) {
    return __builtin_bit_cast(float, __builtin_amdgcn_mov_dpp(__builtin_bit_cast(int, v), CTRL, 0xf, 0xf, true));
}
constexpr int XOR1 = 0xB1, XOR2 = 0x4E, XOR7 = 0x141, XOR8 = 0x128;
__device__ __forceinline__ float fold32(float a, float b) {
    auto s = __builtin_amdgcn_permlane32_swap(__float_as_uint(a), __float_as_uint(b), false, false);
    return __uint_as_float(s[0]) + __uint_as_float(s[1]);
}
__device__ __forceinline__ float fold16(float a, float b) {
    auto s = __builtin_amdgcn_permlane16_swap(__float_as_uint(a), __float_as_uint(b), false, false);
    return __uint_as_float(s[0]) + __uint_as_float(s[1]);
}
__device__ __forceinline__ float wave_sum(float v) {
    v += dpp<XOR1>(v); v += dpp<XOR2>(v); v += dpp<XOR7>(v); v += dpp<XOR8>(v);
    v = fold16(v, v); return fold32(v, v);
}
__device__ __forceinline__ float wave_max(float v) {
    v = fmaxf(v, dpp<XOR1>(v)); v = fmaxf(v, dpp<XOR2>(v)); v = fmaxf(v, dpp<XOR7>(v)); v = fmaxf(v, dpp<XOR8>(v));
    auto s = __builtin_amdgcn_permlane16_swap(__float_as_uint(v), __float_as_uint(v), false, false);
    v = fmaxf(__uint_as_float(s[0]), __uint_as_float(s[1]));
    auto t = __builtin_amdgcn_permlane32_swap(__float_as_uint(v), __float_as_uint(v), false, false);
    return fmaxf(__uint_as_float(t[0]), __uint_as_float(t[1]));
}
__device__ __forceinline__ v2f fold_rows(const float (&a)[8]) {
    const float x0 = fold32(a[0], a[4]), x1 = fold32(a[1], a[5]), x2 = fold32(a[2], a[6]), x3 = fold32(a[3], a[7]);
    v2f o; o.x = fold16(x0, x2); o.y = fold16(x1, x3); return o;
}
__device__ __forceinline__ void ln8(const float (&v)[8], float (&o)[8]) {
    float s = 0.f;
#pragma unroll
    for (int i = 0; i < 8; ++i) s += v[i];
    const float mean = wave_sum(s) * (1.f / DM);
    float q = 0.f;
#pragma unroll
    for (int i = 0; i < 8; ++i) { const float d = v[i] - mean; q = fmaf(d, d, q); }
    const float var = bfr(wave_sum(q) * (1.f / DM));
    const float m = bfr(mean);
    const float den = bfr(__builtin_amdgcn_sqrtf(bfr(var + 1e-5f)));
    const float rc = __builtin_amdgcn_rcpf(den);
#pragma unroll
    for (int i = 0; i < 8; ++i) {
        const float d = bfr(v[i] - m), t = d * rc;
        o[i] = fmaf(fmaf(-t, den, d), rc, t);
    }
}
__device__ __forceinline__ void unpack8(v4u u, float (&v)[8]) {
#pragma unroll
    for (int e = 0; e < 4; ++e) { v[2 * e] = bf_lo(u[e]); v[2 * e + 1] = bf_hi(u[e]); }
}
__device__ __forceinline__ void store16_agent(GAS char* p, v4u v) {
    asm volatile("global_store_dwordx4 %0, %1, off sc1\n\ts_nop 1" :: "v"(p), "v"(v) : "memory");
}
#define LGKM0() asm volatile("s_waitcnt lgkmcnt(0)" ::: "memory")
#define CFENCE() asm volatile("" ::: "memory")
#define PIN16(w, o) asm volatile("" : "+v"(w[o + 0]), "+v"(w[o + 1]), "+v"(w[o + 2]), "+v"(w[o + 3]), "+v"(w[o + 4]), "+v"(w[o + 5]), "+v"(w[o + 6]), "+v"(w[o + 7]), \
                                      "+v"(w[o + 8]), "+v"(w[o + 9]), "+v"(w[o + 10]), "+v"(w[o + 11]), "+v"(w[o + 12]), "+v"(w[o + 13]), "+v"(w[o + 14]), "+v"(w[o + 15]) :: "memory")

__device__ __forceinline__ void pair_rows(v4u (&w)[32]) {
#pragma unroll
    for (int i = 0; i < 16; ++i)
#pragma unroll
        for (int e = 0; e < 4; ++e) {
            const u32 a = w[2 * i][e], b = w[2 * i + 1][e];
            w[2 * i][e] = __builtin_amdgcn_perm(b, a, 0x05040100u);
            w[2 * i + 1][e] = __builtin_amdgcn_perm(b, a, 0x07060302u);
        }
}
__device__ __forceinline__ v2f gemv32(const v4u (&w)[32], const LAS v4u* vec  ) {
    v4u p[4];
#pragma unroll
    for (int j = 0; j < 4; ++j) p[j] = vec[j];
    float a[8];
#pragma unroll
    for (int e = 0; e < 8; ++e) a[e] = 0.f;
#pragma unroll
    for (int i = 0; i < 16; ++i)
#pragma unroll
        for (int e = 0; e < 4; ++e) {
            a[2 * e] = dot2(w[2 * i][e], p[i >> 2][i & 3], a[2 * e]);
            a[2 * e + 1] = dot2(w[2 * i + 1][e], p[i >> 2][i & 3], a[2 * e + 1]);
        }
    return fold_rows(a);
}
__device__ __forceinline__ void octet_rows(v4u (&w)[32]) {
    v4u o[32];
#pragma unroll
    for (int j = 0; j < 4; ++j)
#pragma unroll
        for (int t = 0; t < 4; ++t)
#pragma unroll
            for (int e = 0; e < 4; ++e) {
                const u32 a = w[8 * j + 2 * t][e], b = w[8 * j + 2 * t + 1][e];
                o[4 * (2 * e) + j][t] = __builtin_amdgcn_perm(b, a, 0x05040100u);
                o[4 * (2 * e + 1) + j][t] = __builtin_amdgcn_perm(b, a, 0x07060302u);
            }
#pragma unroll
    for (int i = 0; i < 32; ++i) w[i] = o[i];
}
template <int J> __device__ __forceinline__ v4u bcast4(u32 pr) {
    return (v4u){(u32)__builtin_amdgcn_mov_dpp(pr, 0x150 + 4 * J, 0xf, 0xf, true), (u32)__builtin_amdgcn_mov_dpp(pr, 0x151 + 4 * J, 0xf, 0xf, true),
                 (u32)__builtin_amdgcn_mov_dpp(pr, 0x152 + 4 * J, 0xf, 0xf, true), (u32)__builtin_amdgcn_mov_dpp(pr, 0x153 + 4 * J, 0xf, 0xf, true)};
}
__device__ __forceinline__ v2f gemv32_mfma(const v4u (&w)[32], u32 pr, bool r1, bool r2) {
    const v4u p[4] = {bcast4<0>(pr), bcast4<1>(pr), bcast4<2>(pr), bcast4<3>(pr)};
    v4f a[8];
#pragma unroll
    for (int e = 0; e < 8; ++e) a[e] = (v4f){0.f, 0.f, 0.f, 0.f};
#pragma unroll
    for (int j = 0; j < 4; ++j)
#pragma unroll
        for (int e = 0; e < 8; ++e)
            a[e] = __builtin_amdgcn_mfma_f32_16x16x32_bf16(__builtin_bit_cast(bf16x8, p[j]), __builtin_bit_cast(bf16x8, w[4 * e + j]), a[e], 0, 0, 0);
    v2f o;
    o.x = r2 ? (r1 ? a[6].x : a[4].x) : (r1 ? a[2].x : a[0].x);
    o.y = r2 ? (r1 ? a[7].x : a[5].x) : (r1 ? a[3].x : a[1].x);
    return o;
}
__device__ __forceinline__ void cbar(LAS char* L, int which, int ci, u32& budget) {
    LGKM0();
    *(volatile LAS u32*)(L + CT_FLAG + which * 16 + ci * 4) = 1u;
    for (;;) {
        const v4u f = *(volatile LAS v4u*)(L + CT_FLAG + which * 16);
        if (__builtin_amdgcn_readfirstlane(f.x & f.y & f.z & f.w)) break;
        if (budget == 0) break;
        --budget;
    }
    CFENCE();
}
__device__ __forceinline__ void wait_landed(LAS char* L, u32 tile, u32& landed, u32& budget) {
    while (landed <= tile) {
        landed = __builtin_amdgcn_readfirstlane(*(volatile LAS u32*)(L + CT_LANDED));
        if (landed > tile || budget == 0) break;
        --budget;
        __builtin_amdgcn_s_sleep(1);
    }
    CFENCE();
}

constexpr int ABIT[13] = {8, 10, 13, 16, 9, 11, 19, 15, 18, 17, 20, 12, 14};
constexpr u32 abits(u32 v, int first, int n) {
    u32 o = 0;
    for (int i = 0; i < n; ++i) o |= (v >> i & 1) << ABIT[first + i];
    return o;
}
static_assert(abits(0x1fff, 0, 13) == 0x1fff00, "a permutation of the row bits");
template <int FIRST, int N> __device__ __forceinline__ u32 abits_of(u32 v) {
    u32 o = 0;
#pragma unroll
    for (int i = 0; i < N; ++i) o |= (v >> i & 1) << ABIT[FIRST + i];
    return o;
}
constexpr u64 PRE_OFFS = (u64)(abits(1, 4, 9) >> 8) | (u64)(abits(2, 4, 9) >> 8) << 16 | (u64)(abits(3, 4, 9) >> 8) << 32 | (u64)(abits(4, 4, 9) >> 8) << 48;
static_assert(PRE <= 5 && RUNLEN == 16, "prologue offsets, run table");
constexpr int LAGE = (WMAX + 3) / 4;
static_assert(RING_OFF == 0 && NSLOT * TILE == 0x20000 && WMAX + 4 <= 63 && WMAX == WMIN + 5 && 4 * PRE <= 63, "loader loop constants");
__device__ __forceinline__ void stream_tiles(u32 win, const GAS char* src, u32 tab, u32 li, const u32 (&vo)[4], u32 t, u32 end, u32 pub_addr) {
    u32 tmp, n = end - t, pub = t - ((win + 3) >> 2);
    const u32 slot = RING_OFF + (t & (NSLOT - 1)) * TILE;
    const u32 skip = 4 * (win - WMIN);
    const u32 slo = (u32)(size_t)src, shi = (u32)((size_t)src >> 32);
    asm volatile("v_readlane_b32 s44, %[tab], %[li]\n\t"
                 "s_getpc_b64 s[40:41]\n"
                 "1:\n\t"
                 "s_add_u32 s40, s40, %[skip]\n\t"
                 "s_addc_u32 s41, s41, 0\n\t"
                 "s_add_u32 s40, s40, 2f-1b\n\t"
                 "s_addc_u32 s41, s41, 0\n\t"
                 "s_mov_b32 m0, %[slot]\n\t"
                 "s_add_u32 s42, %[slo], s44\n\t"
                 "s_addc_u32 s43, %[shi], 0\n\t"
                 "s_setpc_b64 s[40:41]\n"
                 "2:\n\t"
                 "s_waitcnt vmcnt(%c[w0])\n\t"
                 "s_waitcnt vmcnt(%c[w1])\n\t"
                 "s_waitcnt vmcnt(%c[w2])\n\t"
                 "s_waitcnt vmcnt(%c[w3])\n\t"
                 "s_waitcnt vmcnt(%c[w4])\n\t"
                 "s_waitcnt vmcnt(%c[w5])\n\t"
                 "global_load_lds_dwordx4 %[v0], s[42:43] nt\n\t"
                 "global_load_lds_dwordx4 %[v1], s[42:43] offset:1024 nt\n\t"
                 "global_load_lds_dwordx4 %[v2], s[42:43] offset:2048 nt\n\t"
                 "global_load_lds_dwordx4 %[v3], s[42:43] offset:3072 nt\n\t"
                 "v_max_i32 %[tmp], 0, %[pub]\n\t"
                 "s_add_u32 %[li], %[li], 1\n\t"
                 "ds_write_b32 %[pa], %[tmp]\n\t"
                 "v_add_u32 %[pub], 1, %[pub]\n\t"
                 "v_readlane_b32 s44, %[tab], %[li]\n\t"
                 "s_add_u32 m0, m0, 0x1000\n\t"
                 "s_and_b32 m0, m0, 0x1f000\n\t"
                 "s_add_u32 s42, %[slo], s44\n\t"
                 "s_addc_u32 s43, %[shi], 0\n\t"
                 "s_sub_u32 %[n], %[n], 1\n\t"
                 "s_cmp_eq_u32 %[n], 0\n\t"
                 "s_cbranch_scc1 3f\n\t"
                 "s_setpc_b64 s[40:41]\n"
                 "3:"
                 : [pub] "+v"(pub), [n] "+s"(n), [li] "+s"(li), [tmp] "=&v"(tmp)
                 : [slo] "s"(slo), [shi] "s"(shi), [pa] "v"(pub_addr), [slot] "s"(slot), [skip] "s"(skip), [tab] "v"(tab),
                   [v0] "v"(vo[0]), [v1] "v"(vo[1]), [v2] "v"(vo[2]), [v3] "v"(vo[3]),
                   [w0] "i"(WMIN), [w1] "i"(WMIN + 1), [w2] "i"(WMIN + 2), [w3] "i"(WMIN + 3), [w4] "i"(WMIN + 4), [w5] "i"(WMIN + 5)
                 : "scc", "s40", "s41", "s42", "s43", "s44", "memory");
}
template <int N> __device__ __forceinline__ void publish_tail(u32 pub_addr) {
    asm volatile("s_waitcnt vmcnt(%c2)\n\tds_write_b32 %0, %1" :: "v"(pub_addr), "v"(2 * TPH - N), "i"(4 * N) : "memory");
    if constexpr (N > 0) publish_tail<N - 1>(pub_addr);
}

__global__ void __launch_bounds__(384, 2)
fused_decode_kernel(const char* __restrict__ kc_, const char* __restrict__ wq_, const char* __restrict__ x_, char* ws_,
                    const char* __restrict__ vc_, const char* __restrict__ wo_,
                    const char* __restrict__ w1_, const char* __restrict__ b1_, const char* __restrict__ w2_,
                    const char* __restrict__ b2_, u16* __restrict__ out_) {
    const GAS char* x = (const GAS char*)x_; const GAS char* kc = (const GAS char*)kc_; const GAS char* vc = (const GAS char*)vc_;
    const GAS char* w1 = (const GAS char*)w1_; const GAS char* b1 = (const GAS char*)b1_; const GAS char* w2 = (const GAS char*)w2_;
    const GAS char* b2 = (const GAS char*)b2_; GAS u16* out = (GAS u16*)out_; GAS char* ws = (GAS char*)ws_;
    __shared__ __attribute__((aligned(1024))) char smem[LDS_BYTES];
    LAS char* L = (LAS char*)smem;
    const int lane = threadIdx.x & 63;
    const int wave = __builtin_amdgcn_readfirstlane(threadIdx.x >> 6);
    const int h = blockIdx.x & 31, b = blockIdx.x >> 5;
    const int wg = b * 32 + h;
    u32 budget = SPIN_CAP;

    const GAS char* src = kc + (size_t)wg * (SK * DH * 2) - 3072;
    u32 vo[4];
#pragma unroll
    for (int g = 0; g < 4; ++g)
        vo[g] = (lane & 15) * 16 + abits_of<0, 2>(lane >> 4) + abits(g, 2, 2) + (3 - g) * 1024;
    {
        u64 pk = PRE_OFFS; u32 xo = 0;
        asm volatile("s_cmp_lg_u32 %[wave], 0\n\t"
                     "s_cbranch_scc1 2f\n\t"
                     "s_mov_b32 m0, 0\n\t"
                     "s_setprio 3\n"
                     "1:\n\t"
                     "s_add_u32 s42, %[slo], %[x]\n\t"
                     "s_addc_u32 s43, %[shi], 0\n\t"
                     "s_and_b32 %[x], s44, 0xffff\n\t"
                     "global_load_lds_dwordx4 %[v0], s[42:43] nt\n\t"
                     "global_load_lds_dwordx4 %[v1], s[42:43] offset:1024 nt\n\t"
                     "global_load_lds_dwordx4 %[v2], s[42:43] offset:2048 nt\n\t"
                     "global_load_lds_dwordx4 %[v3], s[42:43] offset:3072 nt\n\t"
                     "s_add_u32 m0, m0, 0x1000\n\t"
                     "s_lshl_b32 %[x], %[x], 8\n\t"
                     "s_lshr_b64 s[44:45], s[44:45], 16\n\t"
                     "s_cmp_lg_u32 m0, %c[end]\n\t"
                     "s_cbranch_scc1 1b\n"
                     "2:"
                     : [x] "+s"(xo), "+{s[44:45]}"(pk) : [slo] "s"((u32)(size_t)src), [shi] "s"((u32)((size_t)src >> 32)), [wave] "s"(wave), [end] "i"(PRE * TILE),
                       [v0] "v"(vo[0]), [v1] "v"(vo[1]), [v2] "v"(vo[2]), [v3] "v"(vo[3]), "s"(wq_), "s"(x_), "s"(ws_) : "scc", "s42", "s43", "memory");
    }

    if (wave == 0) {
        const u32 lbase = (u32)(size_t)L;
        asm volatile("s_barrier" ::: "memory");
        const u32 pub_addr = lbase + CT_LANDED + lane * 4;
        const u32 free_addr = lbase + CT_FREE;
        const u32 tlo = abits_of<4, 4>(lane), thi = abits_of<8, 5>(lane);
        u32 t = PRE;
        for (;;) {
            v2u f0, f1; u32 wv;
            asm volatile("ds_read2st64_b32 %0, %3 offset0:0 offset1:1\n\tds_read2st64_b32 %1, %3 offset0:2 offset1:3\n\t"
                         "ds_read_b32 %2, %3 offset:%c4\n\ts_waitcnt lgkmcnt(0)"
                         : "=&v"(f0), "=&v"(f1), "=&v"(wv) : "v"(free_addr), "i"(CT_WIN - CT_FREE) : "memory");
            u32 limit = 4 * __builtin_amdgcn_readfirstlane(min(min(f0.x, f0.y), min(f1.x, f1.y))) + NSLOT;
            const u32 win = __builtin_amdgcn_readfirstlane(min(max(wv, (u32)WMIN), (u32)WMAX));
            if (t >= limit) {
                if (budget != 0) { --budget; __builtin_amdgcn_s_sleep(1); continue; }
                limit = ~0u;
            }
            const u32 end = min(limit, (t | (RUNLEN - 1)) + 1);
            stream_tiles(win, src + (u32)__builtin_amdgcn_readlane(thi, (t >> 4) & 31), tlo, t & 15, vo, t, end, pub_addr);
            t = end;
            if (t == TPH) src = vc + (size_t)wg * (SK * DH * 2) - 3072;
            if (t == 2 * TPH) break;
        }
        publish_tail<LAGE - 1>(pub_addr);
        asm volatile("s_waitcnt lgkmcnt(0)\n\ts_barrier" ::: "memory");
    } else if (wave == 5) {
        {
            const v4u z = {0u, 0u, 0u, 0u};
            volatile LAS v4u* cw = (volatile LAS v4u*)(L + CT_OFF);
            cw[lane] = z;
            if (lane < CT_BYTES / 16 - 64) cw[64 + lane] = z;
            *(volatile LAS u32*)(L + CT_WIN) = WAITN;
        }
        LGKM0();
        asm volatile("s_barrier" ::: "memory");
        __builtin_amdgcn_sched_barrier(0);
        const v4u xr = *(const GAS v4u*)(x + b * (DM * 2) + lane * 16);
        const u32 xo = *(const GAS u16*)(x + (b * DM + 16 * h + (lane & 15)) * 2);
        const u32 b2r = *(const GAS u16*)b2;
        const u32 tag0 = __hip_atomic_load((GAS u32*)(ws + WS_EPOCH), RLX_AGENT) + TAG_STEP;
        v2u w1r[7], b1r[7], w2r[7];
#pragma unroll
        for (int k = 0; k < 7; ++k) {
            w1r[k] = *(const GAS v2u*)(w1 + lane * 56 + k * 8);
            b1r[k] = *(const GAS v2u*)(b1 + lane * 56 + k * 8);
            w2r[k] = *(const GAS v2u*)(w2 + lane * 56 + k * 8);
        }
        {
            float xv[8], xn[8], hv[8];
            unpack8(xr, xv);
            ln8(xv, xn);
#pragma unroll
            for (int i = 0; i < 8; ++i) xn[i] = bfr(xn[i]);
            ln8(xn, hv);
#pragma unroll
            for (int i = 0; i < 8; ++i) hv[i] = bfr(hv[i]);
            v4f a = {hv[0], hv[1], hv[2], hv[3]}, bb = {hv[4], hv[5], hv[6], hv[7]};
            *(LAS v4f*)(L + GH_OFF + lane * 32) = a;
            *(LAS v4f*)(L + GH_OFF + lane * 32 + 16) = bb;
        }
        float gate = 0.f;
#pragma unroll 1
        for (int d = 0; d < 16; ++d) {
            const float hd = *(const LAS float*)(L + GH_OFF + (16 * h + d) * 4);
            float y = 0.f;
#pragma unroll
            for (int k = 0; k < 14; ++k) {
                const u32 ww = w1r[k >> 1][k & 1], bb = b1r[k >> 1][k & 1], w2 = w2r[k >> 1][k & 1];
                const u32 t = pk_bf16(hd * bf_lo(ww), hd * bf_hi(ww));
                const u32 uu = pk_bf16(bf_lo(t) + bf_lo(bb), bf_hi(t) + bf_hi(bb));
                const float u0 = bf_lo(uu), u1 = bf_hi(uu);
                constexpr float C0 = -2.f * 0.7978845608028654f * 1.4426950408889634f, C1 = C0 * 0.044715f;
                const float z0 = u0 * fmaf(u0 * u0, C1, C0), z1 = u1 * fmaf(u1 * u1, C1, C0);
                const float g0 = u0 * __builtin_amdgcn_rcpf(1.f + __builtin_amdgcn_exp2f(z0));
                const float g1 = u1 * __builtin_amdgcn_rcpf(1.f + __builtin_amdgcn_exp2f(z1));
                y = dot2(pk_bf16(g0, g1), w2, y);
            }
            y = bfr(bfr(wave_sum(y)) + bf_lo(b2r));
            const float gd = bfr(y / (1.f + __expf(-y)));
            if ((lane & 15) == d) gate = gd;
        }
        {
            u32 ref = 0, have = 0, per = (u32)__builtin_amdgcn_s_memrealtime() >> PSHIFT;
            for (;;) {
                const u32 ld = __builtin_amdgcn_readfirstlane(*(volatile LAS u32*)(L + CT_LANDED));
                if (ld >= 2 * TPH - CSTOP || budget == 0) break;
                --budget;
                const u32 now = (u32)__builtin_amdgcn_s_memrealtime();
                const u32 lag = now * 10u - ld * TAU;
                if ((now >> PSHIFT) != per) {
                    per = now >> PSHIFT;
                    GAS char* pw = ws + WS_PROG + (per & 63) * (8 * PSTRIDE), *pr = ws + WS_PROG + ((per - 1) & 63) * (8 * PSTRIDE);
                    if (b == 0 && lane == 0) __hip_atomic_store((GAS u64*)(pw + (h & 7) * PSTRIDE + (h >> 3) * 8), ((u64)(tag0 + per) << 32) | lag, RLX_AGENT);
                    const u64 e = __hip_atomic_load((GAS u64*)(pr + (lane & 7) * PSTRIDE + ((lane >> 3) & 3) * 8), RLX_AGENT);
                    const bool ok = (u32)(e >> 32) == tag0 + per - 1;
                    const int n = __builtin_popcountll(__ballot(ok));
                    const float d = wave_sum(ok ? (float)(int)((u32)e - lag) : 0.f);
                    if (n >= 48) { ref = lag + __builtin_amdgcn_readfirstlane((int)(d / (float)n)); have = 1; }
                }
                if (have) {
                    const float dev = (float)(int)(lag - ref);
                    const float wf = fminf(fmaxf((float)WAITN + __builtin_rintf(dev * (1.f / CGAIN)), (float)WMIN), (float)WMAX);
                    *(volatile LAS u32*)(L + CT_WIN) = (u32)wf;
                }
                __builtin_amdgcn_s_sleep(32);
            }
        }
        asm volatile("s_barrier" ::: "memory");
        __builtin_amdgcn_s_sleep(28);
        CFENCE();
        __builtin_amdgcn_s_setprio(3);
        const u32 tag = __builtin_amdgcn_readfirstlane(*(volatile LAS u32*)(L + CT_TAG));
        GAS u64* in = (GAS u64*)(ws + WS_INBOX + (size_t)wg * 4096) + lane;
        u64 v[8];
#pragma unroll
        for (int k = 0; k < 8; ++k) v[k] = 0;
        u32 pend = 0xffu;
        for (;;) {
#pragma unroll
            for (int k = 0; k < 8; ++k)
                if (pend & (1u << k)) v[k] = __hip_atomic_load(in + k * 64, RLX_AGENT);
#pragma unroll
            for (int k = 0; k < 8; ++k)
                if (__all((u32)(v[k] >> 32) == tag)) pend &= ~(1u << k);
            if (pend == 0 || budget == 0) break;
            --budget;
        }
        float s = 0.f;
#pragma unroll
        for (int k = 0; k < 8; ++k) s += __uint_as_float((u32)v[k]);
        s = fold16(s, s);
        s = fold32(s, s);
        const float res = bf_lo(xo) + bfr(bfr(s) * gate);
        if (lane < 16) out[b * DM + 16 * h + lane] = (u16)pk_bf16(res, res);
        if (h == 0 && lane == 0) __hip_atomic_store((GAS u32*)(ws + WS_EPOCH + b * 128), tag, RLX_AGENT);
    } else {
        __builtin_amdgcn_s_setprio(2);
        const int ci = wave - 1, r = lane >> 4, c = lane & 15;
        v4u xr = *(const GAS v4u*)(x + b * (DM * 2) + lane * 16);
        u32 tag = __hip_atomic_load((GAS u32*)(ws + WS_EPOCH + b * 128), RLX_AGENT);
        v4u w[32];
        const int rot = b;
        {
            const __amdgpu_buffer_rsrc_t rs = __builtin_amdgcn_make_buffer_rsrc((void*)wq_, 0, DM * NH * DH * 2, 0x00020000);
            const u32 vo = 32 * r * (NH * DH * 2) + c * 16, so = 128 * ci * (NH * DH * 2) + h * (DH * 2);
#pragma unroll
            for (int i = 0; i < 32; ++i)
                w[i] = __builtin_amdgcn_raw_buffer_load_b128(rs, vo, so + ((i + 4 * rot) & 31) * (NH * DH * 2), AUX_NT);
        }
        __builtin_amdgcn_sched_barrier(0);
        asm volatile("s_barrier" ::: "memory");
        asm volatile("" : "+v"(xr), "+v"(tag) :: "memory");
        tag += TAG_STEP;
        LAS char* priv = L + PV_OFF + ci * 1024;
        {
            float xv[8], xn[8];
            unpack8(xr, xv);
            ln8(xv, xn);
            v4u o;
#pragma unroll
            for (int e = 0; e < 4; ++e) o[e] = pk_bf16(xn[2 * e], xn[2 * e + 1]);
            v2u o0 = {o.x, o.y}, o1 = {o.z, o.w};
            *(LAS v2u*)(priv + (lane >> 2) * 64 + ((4 * (lane & 3) - 2 * rot) & 15) * 4) = o0;
            *(LAS v2u*)(priv + (lane >> 2) * 64 + ((4 * (lane & 3) + 2 - 2 * rot) & 15) * 4) = o1;
        }
        if (ci == 0) *(volatile LAS u32*)(L + CT_TAG) = tag;
        pair_rows(w);
        {
            const v2f qp = gemv32(w, (const LAS v4u*)(priv + (128 * ci + 32 * r) * 2));
            *(LAS v2f*)(L + QP_OFF + ci * 512 + (8 * c + 2 * r) * 4) = qp;
        }
        CFENCE();
        cbar(L, 0, ci, budget);
        u32 q[4];
        {
            v4f s0 = *(const LAS v4f*)(L + QP_OFF + c * 32), s1 = *(const LAS v4f*)(L + QP_OFF + c * 32 + 16);
#pragma unroll
            for (int k = 1; k < 4; ++k) { s0 += *(const LAS v4f*)(L + QP_OFF + k * 512 + c * 32); s1 += *(const LAS v4f*)(L + QP_OFF + k * 512 + c * 32 + 16); }
            q[0] = pk_bf16(s0.x, s0.y); q[1] = pk_bf16(s0.z, s0.w); q[2] = pk_bf16(s1.x, s1.y); q[3] = pk_bf16(s1.z, s1.w);
        }
        tag = *(volatile LAS u32*)(L + CT_TAG);
        volatile LAS u32* freew = (volatile LAS u32*)(L + CT_FREE + ci * 256 + lane * 4);
        LAS char* myscores = L + SC_OFF + ci * 4096;
        u32 landed = 0;
        float mx = -INFINITY;
        const bool lo8 = c < 8, lo4 = (c & 4) == 0;
        const __amdgpu_buffer_rsrc_t wors = __builtin_amdgcn_make_buffer_rsrc((void*)wo_, 0, DM * NH * DH * 2, 0x00020000);
        const u32 wovo = (h * DH + 32 * r) * (DM * 2) + (128 * ci + 8 * c) * 2;
#pragma unroll
        for (int seg = 0; seg < 8; ++seg) {
#pragma unroll
            for (int i = 4 * seg; i < 4 * seg + 4; ++i) {
                const u32 vo = wovo + (i & 3) * (DM * 2), so = ((seg + rot) & 7) * (4 * DM * 2);
                if (seg) w[i] = __builtin_amdgcn_raw_buffer_load_b128(wors, vo, so, 0);
                else w[i] = __builtin_amdgcn_raw_buffer_load_b128(wors, vo, so, AUX_NT);
            }
            if (seg == 0) {
                __builtin_amdgcn_sched_barrier(0);
#pragma unroll
                for (int i = 0; i < 4; ++i) w[28 + i] = __builtin_amdgcn_raw_buffer_load_b128(wors, wovo + i * (DM * 2), rot * (4 * DM * 2), AUX_SC0);
            }
            if (seg == 1) asm volatile("" :: "v"(w[28]), "v"(w[29]), "v"(w[30]), "v"(w[31]));
            __builtin_amdgcn_sched_barrier(0);
#pragma unroll 1
            for (int it = seg * (ITERS / 8); it < (seg + 1) * (ITERS / 8); ++it) {
                const u32 tile = 4 * it + ci;
                wait_landed(L, tile, landed, budget);
                const LAS v4u* tp = (const LAS v4u*)(L + RING_OFF + (tile & (NSLOT - 1)) * TILE + lane * 16);
                v4u k0 = tp[0], k1 = tp[64], k2 = tp[128], k3 = tp[192];
                LGKM0();
                *freew = it + 1;
                asm volatile("" : "+v"(k0), "+v"(k1), "+v"(k2), "+v"(k3) :: "memory");
                float a0 = 0.f, a1 = 0.f, a2 = 0.f, a3 = 0.f;
#pragma unroll
                for (int e = 0; e < 4; ++e) { a0 = dot2(k0[e], q[e], a0); a1 = dot2(k1[e], q[e], a1); a2 = dot2(k2[e], q[e], a2); a3 = dot2(k3[e], q[e], a3); }
                const float b0 = (lo8 ? a0 : a2) + dpp<XOR8>(lo8 ? a2 : a0);
                const float b1 = (lo8 ? a1 : a3) + dpp<XOR8>(lo8 ? a3 : a1);
                float s = (lo4 ? b0 : b1) + dpp<XOR7>(lo4 ? b1 : b0);
                s += dpp<XOR2>(s);
                s += dpp<XOR1>(s);
                const float sc = bfr(s) * 0.088388347648318440f;
                const u32 sb = pk_bf16(sc, sc);
                mx = fmaxf(mx, bf_lo(sb));
                *(LAS u16*)(myscores + it * 32 + r * 8 + (c >> 2) * 2) = (u16)sb;
            }
        }
        octet_rows(w);
        PIN16(w, 0); PIN16(w, 16);
        mx = wave_max(mx);
        *(volatile LAS float*)(L + CT_MAXV + ci * 4) = mx;
        cbar(L, 1, ci, budget);
        {
            const v4f mv = *(volatile LAS v4f*)(L + CT_MAXV);
            const float gm = fmaxf(fmaxf(mv.x, mv.y), fmaxf(mv.z, mv.w));
            LAS v4u* sp = (LAS v4u*)(myscores + lane * 16);
            float ex[32];
            float sum = 0.f;
#pragma unroll
            for (int j = 0; j < 4; ++j) {
                const v4u u = sp[j * 64];
#pragma unroll
                for (int e = 0; e < 4; ++e) {
                    const float e0 = __builtin_amdgcn_exp2f((bf_lo(u[e]) - gm) * 1.4426950408889634f);
                    const float e1 = __builtin_amdgcn_exp2f((bf_hi(u[e]) - gm) * 1.4426950408889634f);
                    ex[8 * j + 2 * e] = e0; ex[8 * j + 2 * e + 1] = e1;
                    sum += e0 + e1;
                }
            }
            sum = wave_sum(sum);
            *(volatile LAS float*)(L + CT_SUMV + ci * 4) = sum;
            cbar(L, 2, ci, budget);
            const v4f sv = *(volatile LAS v4f*)(L + CT_SUMV);
            const float inv = 1.f / ((sv.x + sv.y) + (sv.z + sv.w));
#pragma unroll
            for (int j = 0; j < 4; ++j) {
                v4u u;
#pragma unroll
                for (int e = 0; e < 4; ++e) u[e] = pk_bf16(ex[8 * j + 2 * e] * inv, ex[8 * j + 2 * e + 1] * inv);
                sp[j * 64] = u;
            }
        }
        CFENCE();
        float acc[8];
#pragma unroll
        for (int e = 0; e < 8; ++e) acc[e] = 0.f;
        u32 opw = OP_OFF + ci * 512 + (8 * c + 2 * r) * 4, opr = OP_OFF + (16 * r + ((c + 2 * rot) & 15)) * 8;
        GAS char* g = ws + WS_INBOX + ((size_t)((b * 32 + 8 * ci + (c >> 1)) * 32 + (h & 7) * 4 + (h >> 3))) * 128 + (8 * (c & 1) + 2 * r) * 8;
        asm volatile("" : "+v"(opw), "+v"(opr), "+v"(g));
#pragma unroll 1
        for (int it = 0; it < ITERS; ++it) {
            const u32 tile = TPH + 4 * it + ci;
            const v2u pp = *(const LAS v2u*)(myscores + it * 32 + r * 8);
            wait_landed(L, tile, landed, budget);
            const LAS v4u* tp = (const LAS v4u*)(L + RING_OFF + (tile & (NSLOT - 1)) * TILE + lane * 16);
            v4u v0 = tp[0], v1 = tp[64], v2 = tp[128], v3 = tp[192];
            LGKM0();
            *freew = ITERS + it + 1;
            asm volatile("" : "+v"(v0), "+v"(v1), "+v"(v2), "+v"(v3) :: "memory");
#pragma unroll
            for (int e = 0; e < 4; ++e) {
                acc[2 * e] = dot2(__builtin_amdgcn_perm(v1[e], v0[e], 0x05040100u), pp.x, acc[2 * e]);
                acc[2 * e + 1] = dot2(__builtin_amdgcn_perm(v1[e], v0[e], 0x07060302u), pp.x, acc[2 * e + 1]);
            }
#pragma unroll
            for (int e = 0; e < 4; ++e) {
                acc[2 * e] = dot2(__builtin_amdgcn_perm(v3[e], v2[e], 0x05040100u), pp.y, acc[2 * e]);
                acc[2 * e + 1] = dot2(__builtin_amdgcn_perm(v3[e], v2[e], 0x07060302u), pp.y, acc[2 * e + 1]);
            }
        }
        *(LAS v2f*)(size_t)opw = fold_rows(acc);
        asm volatile("s_waitcnt lgkmcnt(0)\n\ts_barrier" ::: "memory");
        const LAS char* op = (const LAS char*)(size_t)opr;
        const v2f o0 = *(const LAS v2f*)op, o1 = *(const LAS v2f*)(op + 512), o2 = *(const LAS v2f*)(op + 1024), o3 = *(const LAS v2f*)(op + 1536);
        const v2f o = (o0 + o1) + (o2 + o3);
        const v2f sp = gemv32_mfma(w, pk_bf16(o.x, o.y), r & 1, r & 2);
        store16_agent(g, (v4u){__float_as_uint(sp.x), tag, __float_as_uint(sp.y), tag});
    }
}

extern "C" void kernel_launch(void* const* d_in, const int* in_sizes, int n_in,
                              void* d_out, int out_size, void* d_ws, size_t ws_size,
                              hipStream_t stream) {
    (void)in_sizes; (void)n_in; (void)out_size; (void)ws_size;
    fprintf(stderr, "PTR x %p K %p V %p wq %p wo %p i5 %p w1 %p b1 %p w2 %p b2 %p out %p ws %p\n", d_in[0], d_in[1], d_in[2], d_in[3], d_in[4], d_in[5], d_in[6], d_in[7], d_in[8], d_in[9], d_out, d_ws);
    { hipPointerAttribute_t a; if (hipPointerGetAttributes(&a, d_in[1]) == hipSuccess) fprintf(stderr, "ATTR type %d dev %d managed %d flags %u\n", (int)a.type, a.device, a.isManaged, a.allocationFlags);
      void* base = 0; size_t sz = 0; if (hipMemGetAddressRange((hipDeviceptr_t*)&base, &sz, d_in[1]) == hipSuccess) fprintf(stderr, "RANGE K %p %zu\n", base, sz);
      if (hipMemGetAddressRange((hipDeviceptr_t*)&base, &sz, d_in[2]) == hipSuccess) fprintf(stderr, "RANGE V %p %zu\n", base, sz);
      if (hipMemGetAddressRange((hipDeviceptr_t*)&base, &sz, d_ws) == hipSuccess) fprintf(stderr, "RANGE ws %p %zu\n", base, sz); }
    fused_decode_kernel<<<NB * NH, 384, 0, stream>>>(
        (const char*)d_in[1], (const char*)d_in[3], (const char*)d_in[0], (char*)d_ws, (const char*)d_in[2], (const char*)d_in[4],
        (const char*)d_in[6], (const char*)d_in[7], (const char*)d_in[8], (const char*)d_in[9], (u16*)d_out);
}
```

```cpp
#define DIAGCODE ((u32)(((va > ka ? va - ka : ka - va) >> 21) & 31))
#include <hip/hip_runtime.h>
#include <hip/hip_bf16.h>
#include <stdint.h>

#define GAS __attribute__((address_space(1)))
#define LAS __attribute__((address_space(3)))
typedef unsigned u32;
typedef unsigned short u16;
typedef unsigned long long u64;
typedef u32 v4u __attribute__((ext_vector_type(4)));
typedef u32 v2u __attribute__((ext_vector_type(2)));
typedef float v4f __attribute__((ext_vector_type(4)));
typedef float v2f __attribute__((ext_vector_type(2)));
typedef __bf16 bf16x2 __attribute__((ext_vector_type(2)));
typedef __bf16 bf16x8 __attribute__((ext_vector_type(8)));

#define RLX_AGENT __ATOMIC_RELAXED, __HIP_MEMORY_SCOPE_AGENT

constexpr int NB = 8, NH = 32, SK = 8192, DH = 128, DM = 512;
constexpr int TILE = 4096, NSLOT = 32, TPH = SK * DH * 2 / TILE;
constexpr int ITERS = TPH / 4;
constexpr int RING_OFF = 0;
constexpr int SC_OFF = RING_OFF + NSLOT * TILE;
constexpr int QP_OFF = SC_OFF + SK * 2;
constexpr int OP_OFF = QP_OFF + 2048;
constexpr int PV_OFF = OP_OFF + 2048;
constexpr int GH_OFF = PV_OFF + 4096;
constexpr int CT_OFF = GH_OFF + 2048;
constexpr int CT_LANDED = CT_OFF;
constexpr int CT_FREE = CT_OFF + 256;
constexpr int CT_FLAG = CT_OFF + 1280;
constexpr int CT_MAXV = CT_FLAG + 64, CT_SUMV = CT_FLAG + 80, CT_TAG = CT_FLAG + 96, CT_WIN = CT_FLAG + 112;
constexpr int CT_BYTES = 1536;
constexpr int LDS_BYTES = CT_OFF + CT_BYTES;
static_assert(LDS_BYTES <= 160 * 1024 && LDS_BYTES > 80 * 1024, "one workgroup per CU");
constexpr size_t WS_INBOX = 0;
constexpr size_t WS_EPOCH = (size_t)NB * 32 * 32 * 128;
constexpr size_t WS_PROG = 4u << 20;
constexpr u32 TAG_STEP = 0x9E3779B9u;
constexpr u32 SPIN_CAP = 1u << 18;
constexpr int WAITN = 19, WMIN = WAITN - 2, WMAX = WAITN + 3;
constexpr int RUNLEN = 16;
constexpr int PRE = 5;
constexpr float CGAIN = 400.f;
constexpr int TAU = 134;
constexpr int PSHIFT = 9, PSTRIDE = 256, CSTOP = 24;
constexpr int AUX_SC0 = 1, AUX_NT = 2;

__device__ __forceinline__ float bf_lo(u32 u) { return __uint_as_float(u << 16); }
__device__ __forceinline__ float bf_hi(u32 u) { return __uint_as_float(u & 0xffff0000u); }
__device__ __forceinline__ u32 pk_bf16(float lo, float hi) {
    bf16x2 v = {(__bf16)lo, (__bf16)hi};
    return __builtin_bit_cast(u32, v);
}
__device__ __forceinline__ float bfr(float v) { return bf_lo(pk_bf16(v, v)); }
__device__ __forceinline__ float dot2(u32 a, u32 b, float acc) {
    return __builtin_amdgcn_fdot2_f32_bf16(__builtin_bit_cast(bf16x2, a), __builtin_bit_cast(bf16x2, b), acc, false);
}
template <int CTRL> __device__ __forceinline__ float dpp(float v) {
    return __builtin_bit_cast(float, __builtin_amdgcn_mov_dpp(__builtin_bit_cast(int, v), CTRL, 0xf, 0xf, true));
}
constexpr int XOR1 = 0xB1, XOR2 = 0x4E, XOR7 = 0x141, XOR8 = 0x128;
__device__ __forceinline__ float fold32(float a, float b) {
    auto s = __builtin_amdgcn_permlane32_swap(__float_as_uint(a), __float_as_uint(b), false, false);
    return __uint_as_float(s[0]) + __uint_as_float(s[1]);
}
__device__ __forceinline__ float fold16(float a, float b) {
    auto s = __builtin_amdgcn_permlane16_swap(__float_as_uint(a), __float_as_uint(b), false, false);
    return __uint_as_float(s[0]) + __uint_as_float(s[1]);
}
__device__ __forceinline__ float wave_sum(float v) {
    v += dpp<XOR1>(v); v += dpp<XOR2>(v); v += dpp<XOR7>(v); v += dpp<XOR8>(v);
    v = fold16(v, v); return fold32(v, v);
}
__device__ __forceinline__ float wave_max(float v) {
    v = fmaxf(v, dpp<XOR1>(v)); v = fmaxf(v, dpp<XOR2>(v)); v = fmaxf(v, dpp<XOR7>(v)); v = fmaxf(v, dpp<XOR8>(v));
    auto s = __builtin_amdgcn_permlane16_swap(__float_as_uint(v), __float_as_uint(v), false, false);
    v = fmaxf(__uint_as_float(s[0]), __uint_as_float(s[1]));
    auto t = __builtin_amdgcn_permlane32_swap(__float_as_uint(v), __float_as_uint(v), false, false);
    return fmaxf(__uint_as_float(t[0]), __uint_as_float(t[1]));
}
__device__ __forceinline__ v2f fold_rows(const float (&a)[8]) {
    const float x0 = fold32(a[0], a[4]), x1 = fold32(a[1], a[5]), x2 = fold32(a[2], a[6]), x3 = fold32(a[3], a[7]);
    v2f o; o.x = fold16(x0, x2); o.y = fold16(x1, x3); return o;
}
__device__ __forceinline__ void ln8(const float (&v)[8], float (&o)[8]) {
    float s = 0.f;
#pragma unroll
    for (int i = 0; i < 8; ++i) s += v[i];
    const float mean = wave_sum(s) * (1.f / DM);
    float q = 0.f;
#pragma unroll
    for (int i = 0; i < 8; ++i) { const float d = v[i] - mean; q = fmaf(d, d, q); }
    const float var = bfr(wave_sum(q) * (1.f / DM));
    const float m = bfr(mean);
    const float den = bfr(__builtin_amdgcn_sqrtf(bfr(var + 1e-5f)));
    const float rc = __builtin_amdgcn_rcpf(den);
#pragma unroll
    for (int i = 0; i < 8; ++i) {
        const float d = bfr(v[i] - m), t = d * rc;
        o[i] = fmaf(fmaf(-t, den, d), rc, t);
    }
}
__device__ __forceinline__ void unpack8(v4u u, float (&v)[8]) {
#pragma unroll
    for (int e = 0; e < 4; ++e) { v[2 * e] = bf_lo(u[e]); v[2 * e + 1] = bf_hi(u[e]); }
}
__device__ __forceinline__ void store16_agent(GAS char* p, v4u v) {
    asm volatile("global_store_dwordx4 %0, %1, off sc1\n\ts_nop 1" :: "v"(p), "v"(v) : "memory");
}
#define LGKM0() asm volatile("s_waitcnt lgkmcnt(0)" ::: "memory")
#define CFENCE() asm volatile("" ::: "memory")
#define PIN16(w, o) asm volatile("" : "+v"(w[o + 0]), "+v"(w[o + 1]), "+v"(w[o + 2]), "+v"(w[o + 3]), "+v"(w[o + 4]), "+v"(w[o + 5]), "+v"(w[o + 6]), "+v"(w[o + 7]), \
                                      "+v"(w[o + 8]), "+v"(w[o + 9]), "+v"(w[o + 10]), "+v"(w[o + 11]), "+v"(w[o + 12]), "+v"(w[o + 13]), "+v"(w[o + 14]), "+v"(w[o + 15]) :: "memory")

__device__ __forceinline__ void pair_rows(v4u (&w)[32]) {
#pragma unroll
    for (int i = 0; i < 16; ++i)
#pragma unroll
        for (int e = 0; e < 4; ++e) {
            const u32 a = w[2 * i][e], b = w[2 * i + 1][e];
            w[2 * i][e] = __builtin_amdgcn_perm(b, a, 0x05040100u);
            w[2 * i + 1][e] = __builtin_amdgcn_perm(b, a, 0x07060302u);
        }
}
__device__ __forceinline__ v2f gemv32(const v4u (&w)[32], const LAS v4u* vec  ) {
    v4u p[4];
#pragma unroll
    for (int j = 0; j < 4; ++j) p[j] = vec[j];
    float a[8];
#pragma unroll
    for (int e = 0; e < 8; ++e) a[e] = 0.f;
#pragma unroll
    for (int i = 0; i < 16; ++i)
#pragma unroll
        for (int e = 0; e < 4; ++e) {
            a[2 * e] = dot2(w[2 * i][e], p[i >> 2][i & 3], a[2 * e]);
            a[2 * e + 1] = dot2(w[2 * i + 1][e], p[i >> 2][i & 3], a[2 * e + 1]);
        }
    return fold_rows(a);
}
__device__ __forceinline__ void octet_rows(v4u (&w)[32]) {
    v4u o[32];
#pragma unroll
    for (int j = 0; j < 4; ++j)
#pragma unroll
        for (int t = 0; t < 4; ++t)
#pragma unroll
            for (int e = 0; e < 4; ++e) {
                const u32 a = w[8 * j + 2 * t][e], b = w[8 * j + 2 * t + 1][e];
                o[4 * (2 * e) + j][t] = __builtin_amdgcn_perm(b, a, 0x05040100u);
                o[4 * (2 * e + 1) + j][t] = __builtin_amdgcn_perm(b, a, 0x07060302u);
            }
#pragma unroll
    for (int i = 0; i < 32; ++i) w[i] = o[i];
}
template <int J> __device__ __forceinline__ v4u bcast4(u32 pr) {
    return (v4u){(u32)__builtin_amdgcn_mov_dpp(pr, 0x150 + 4 * J, 0xf, 0xf, true), (u32)__builtin_amdgcn_mov_dpp(pr, 0x151 + 4 * J, 0xf, 0xf, true),
                 (u32)__builtin_amdgcn_mov_dpp(pr, 0x152 + 4 * J, 0xf, 0xf, true), (u32)__builtin_amdgcn_mov_dpp(pr, 0x153 + 4 * J, 0xf, 0xf, true)};
}
__device__ __forceinline__ v2f gemv32_mfma(const v4u (&w)[32], u32 pr, bool r1, bool r2) {
    const v4u p[4] = {bcast4<0>(pr), bcast4<1>(pr), bcast4<2>(pr), bcast4<3>(pr)};
    v4f a[8];
#pragma unroll
    for (int e = 0; e < 8; ++e) a[e] = (v4f){0.f, 0.f, 0.f, 0.f};
#pragma unroll
    for (int j = 0; j < 4; ++j)
#pragma unroll
        for (int e = 0; e < 8; ++e)
            a[e] = __builtin_amdgcn_mfma_f32_16x16x32_bf16(__builtin_bit_cast(bf16x8, p[j]), __builtin_bit_cast(bf16x8, w[4 * e + j]), a[e], 0, 0, 0);
    v2f o;
    o.x = r2 ? (r1 ? a[6].x : a[4].x) : (r1 ? a[2].x : a[0].x);
    o.y = r2 ? (r1 ? a[7].x : a[5].x) : (r1 ? a[3].x : a[1].x);
    return o;
}
__device__ __forceinline__ void cbar(LAS char* L, int which, int ci, u32& budget) {
    LGKM0();
    *(volatile LAS u32*)(L + CT_FLAG + which * 16 + ci * 4) = 1u;
    for (;;) {
        const v4u f = *(volatile LAS v4u*)(L + CT_FLAG + which * 16);
        if (__builtin_amdgcn_readfirstlane(f.x & f.y & f.z & f.w)) break;
        if (budget == 0) break;
        --budget;
    }
    CFENCE();
}
__device__ __forceinline__ void wait_landed(LAS char* L, u32 tile, u32& landed, u32& budget) {
    while (landed <= tile) {
        landed = __builtin_amdgcn_readfirstlane(*(volatile LAS u32*)(L + CT_LANDED));
        if (landed > tile || budget == 0) break;
        --budget;
        __builtin_amdgcn_s_sleep(1);
    }
    CFENCE();
}

constexpr int ABIT[13] = {8, 10, 13, 16, 9, 11, 19, 15, 18, 17, 20, 12, 14};
constexpr u32 abits(u32 v, int first, int n) {
    u32 o = 0;
    for (int i = 0; i < n; ++i) o |= (v >> i & 1) << ABIT[first + i];
    return o;
}
static_assert(abits(0x1fff, 0, 13) == 0x1fff00, "a permutation of the row bits");
template <int FIRST, int N> __device__ __forceinline__ u32 abits_of(u32 v) {
    u32 o = 0;
#pragma unroll
    for (int i = 0; i < N; ++i) o |= (v >> i & 1) << ABIT[FIRST + i];
    return o;
}
constexpr u64 PRE_OFFS = (u64)(abits(1, 4, 9) >> 8) | (u64)(abits(2, 4, 9) >> 8) << 16 | (u64)(abits(3, 4, 9) >> 8) << 32 | (u64)(abits(4, 4, 9) >> 8) << 48;
static_assert(PRE <= 5 && RUNLEN == 16, "prologue offsets, run table");
constexpr int LAGE = (WMAX + 3) / 4;
static_assert(RING_OFF == 0 && NSLOT * TILE == 0x20000 && WMAX + 4 <= 63 && WMAX == WMIN + 5 && 4 * PRE <= 63, "loader loop constants");
__device__ __forceinline__ void stream_tiles(u32 win, const GAS char* src, u32 tab, u32 li, const u32 (&vo)[4], u32 t, u32 end, u32 pub_addr) {
    u32 tmp, n = end - t, pub = t - ((win + 3) >> 2);
    const u32 slot = RING_OFF + (t & (NSLOT - 1)) * TILE;
    const u32 skip = 4 * (win - WMIN);
    const u32 slo = (u32)(size_t)src, shi = (u32)((size_t)src >> 32);
    asm volatile("v_readlane_b32 s44, %[tab], %[li]\n\t"
                 "s_getpc_b64 s[40:41]\n"
                 "1:\n\t"
                 "s_add_u32 s40, s40, %[skip]\n\t"
                 "s_addc_u32 s41, s41, 0\n\t"
                 "s_add_u32 s40, s40, 2f-1b\n\t"
                 "s_addc_u32 s41, s41, 0\n\t"
                 "s_mov_b32 m0, %[slot]\n\t"
                 "s_add_u32 s42, %[slo], s44\n\t"
                 "s_addc_u32 s43, %[shi], 0\n\t"
                 "s_setpc_b64 s[40:41]\n"
                 "2:\n\t"
                 "s_waitcnt vmcnt(%c[w0])\n\t"
                 "s_waitcnt vmcnt(%c[w1])\n\t"
                 "s_waitcnt vmcnt(%c[w2])\n\t"
                 "s_waitcnt vmcnt(%c[w3])\n\t"
                 "s_waitcnt vmcnt(%c[w4])\n\t"
                 "s_waitcnt vmcnt(%c[w5])\n\t"
                 "global_load_lds_dwordx4 %[v0], s[42:43] nt\n\t"
                 "global_load_lds_dwordx4 %[v1], s[42:43] offset:1024 nt\n\t"
                 "global_load_lds_dwordx4 %[v2], s[42:43] offset:2048 nt\n\t"
                 "global_load_lds_dwordx4 %[v3], s[42:43] offset:3072 nt\n\t"
                 "v_max_i32 %[tmp], 0, %[pub]\n\t"
                 "s_add_u32 %[li], %[li], 1\n\t"
                 "ds_write_b32 %[pa], %[tmp]\n\t"
                 "v_add_u32 %[pub], 1, %[pub]\n\t"
                 "v_readlane_b32 s44, %[tab], %[li]\n\t"
                 "s_add_u32 m0, m0, 0x1000\n\t"
                 "s_and_b32 m0, m0, 0x1f000\n\t"
                 "s_add_u32 s42, %[slo], s44\n\t"
                 "s_addc_u32 s43, %[shi], 0\n\t"
                 "s_sub_u32 %[n], %[n], 1\n\t"
                 "s_cmp_eq_u32 %[n], 0\n\t"
                 "s_cbranch_scc1 3f\n\t"
                 "s_setpc_b64 s[40:41]\n"
                 "3:"
                 : [pub] "+v"(pub), [n] "+s"(n), [li] "+s"(li), [tmp] "=&v"(tmp)
                 : [slo] "s"(slo), [shi] "s"(shi), [pa] "v"(pub_addr), [slot] "s"(slot), [skip] "s"(skip), [tab] "v"(tab),
                   [v0] "v"(vo[0]), [v1] "v"(vo[1]), [v2] "v"(vo[2]), [v3] "v"(vo[3]),
                   [w0] "i"(WMIN), [w1] "i"(WMIN + 1), [w2] "i"(WMIN + 2), [w3] "i"(WMIN + 3), [w4] "i"(WMIN + 4), [w5] "i"(WMIN + 5)
                 : "scc", "s40", "s41", "s42", "s43", "s44", "memory");
}
template <int N> __device__ __forceinline__ void publish_tail(u32 pub_addr) {
    asm volatile("s_waitcnt vmcnt(%c2)\n\tds_write_b32 %0, %1" :: "v"(pub_addr), "v"(2 * TPH - N), "i"(4 * N) : "memory");
    if constexpr (N > 0) publish_tail<N - 1>(pub_addr);
}

__global__ void __launch_bounds__(384, 2)
fused_decode_kernel(const char* __restrict__ kc_, const char* __restrict__ wq_, const char* __restrict__ x_, char* ws_,
                    const char* __restrict__ vc_, const char* __restrict__ wo_,
                    const char* __restrict__ w1_, const char* __restrict__ b1_, const char* __restrict__ w2_,
                    const char* __restrict__ b2_, u16* __restrict__ out_) {
    const GAS char* x = (const GAS char*)x_; const GAS char* kc = (const GAS char*)kc_; const GAS char* vc = (const GAS char*)vc_;
    const GAS char* w1 = (const GAS char*)w1_; const GAS char* b1 = (const GAS char*)b1_; const GAS char* w2 = (const GAS char*)w2_;
    const GAS char* b2 = (const GAS char*)b2_; GAS u16* out = (GAS u16*)out_; GAS char* ws = (GAS char*)ws_;
    __shared__ __attribute__((aligned(1024))) char smem[LDS_BYTES];
    LAS char* L = (LAS char*)smem;
    const int lane = threadIdx.x & 63;
    const int wave = __builtin_amdgcn_readfirstlane(threadIdx.x >> 6);
    const int h = blockIdx.x & 31, b = blockIdx.x >> 5;
    const int wg = b * 32 + h;
    u32 budget = SPIN_CAP;

    const GAS char* src = kc + (size_t)wg * (SK * DH * 2) - 3072;
    u32 vo[4];
#pragma unroll
    for (int g = 0; g < 4; ++g)
        vo[g] = (lane & 15) * 16 + abits_of<0, 2>(lane >> 4) + abits(g, 2, 2) + (3 - g) * 1024;
    {
        u64 pk = PRE_OFFS; u32 xo = 0;
        asm volatile("s_cmp_lg_u32 %[wave], 0\n\t"
                     "s_cbranch_scc1 2f\n\t"
                     "s_mov_b32 m0, 0\n\t"
                     "s_setprio 3\n"
                     "1:\n\t"
                     "s_add_u32 s42, %[slo], %[x]\n\t"
                     "s_addc_u32 s43, %[shi], 0\n\t"
                     "s_and_b32 %[x], s44, 0xffff\n\t"
                     "global_load_lds_dwordx4 %[v0], s[42:43] nt\n\t"
                     "global_load_lds_dwordx4 %[v1], s[42:43] offset:1024 nt\n\t"
                     "global_load_lds_dwordx4 %[v2], s[42:43] offset:2048 nt\n\t"
                     "global_load_lds_dwordx4 %[v3], s[42:43] offset:3072 nt\n\t"
                     "s_add_u32 m0, m0, 0x1000\n\t"
                     "s_lshl_b32 %[x], %[x], 8\n\t"
                     "s_lshr_b64 s[44:45], s[44:45], 16\n\t"
                     "s_cmp_lg_u32 m0, %c[end]\n\t"
                     "s_cbranch_scc1 1b\n"
                     "2:"
                     : [x] "+s"(xo), "+{s[44:45]}"(pk) : [slo] "s"((u32)(size_t)src), [shi] "s"((u32)((size_t)src >> 32)), [wave] "s"(wave), [end] "i"(PRE * TILE),
                       [v0] "v"(vo[0]), [v1] "v"(vo[1]), [v2] "v"(vo[2]), [v3] "v"(vo[3]), "s"(wq_), "s"(x_), "s"(ws_) : "scc", "s42", "s43", "memory");
    }

    if (wave == 0) {
        const u32 lbase = (u32)(size_t)L;
        asm volatile("s_barrier" ::: "memory");
        const u32 pub_addr = lbase + CT_LANDED + lane * 4;
        const u32 free_addr = lbase + CT_FREE;
        const u32 tlo = abits_of<4, 4>(lane), thi = abits_of<8, 5>(lane);
        u32 t = PRE;
        for (;;) {
            v2u f0, f1; u32 wv;
            asm volatile("ds_read2st64_b32 %0, %3 offset0:0 offset1:1\n\tds_read2st64_b32 %1, %3 offset0:2 offset1:3\n\t"
                         "ds_read_b32 %2, %3 offset:%c4\n\ts_waitcnt lgkmcnt(0)"
                         : "=&v"(f0), "=&v"(f1), "=&v"(wv) : "v"(free_addr), "i"(CT_WIN - CT_FREE) : "memory");
            u32 limit = 4 * __builtin_amdgcn_readfirstlane(min(min(f0.x, f0.y), min(f1.x, f1.y))) + NSLOT;
            const u32 win = __builtin_amdgcn_readfirstlane(min(max(wv, (u32)WMIN), (u32)WMAX));
            if (t >= limit) {
                if (budget != 0) { --budget; __builtin_amdgcn_s_sleep(1); continue; }
                limit = ~0u;
            }
            const u32 end = min(limit, (t | (RUNLEN - 1)) + 1);
            stream_tiles(win, src + (u32)__builtin_amdgcn_readlane(thi, (t >> 4) & 31), tlo, t & 15, vo, t, end, pub_addr);
            t = end;
            if (t == TPH) src = vc + (size_t)wg * (SK * DH * 2) - 3072;
            if (t == 2 * TPH) break;
        }
        publish_tail<LAGE - 1>(pub_addr);
        asm volatile("s_waitcnt lgkmcnt(0)\n\ts_barrier" ::: "memory");
    } else if (wave == 5) {
        {
            const v4u z = {0u, 0u, 0u, 0u};
            volatile LAS v4u* cw = (volatile LAS v4u*)(L + CT_OFF);
            cw[lane] = z;
            if (lane < CT_BYTES / 16 - 64) cw[64 + lane] = z;
            *(volatile LAS u32*)(L + CT_WIN) = WAITN;
        }
        LGKM0();
        asm volatile("s_barrier" ::: "memory");
        __builtin_amdgcn_sched_barrier(0);
        const v4u xr = *(const GAS v4u*)(x + b * (DM * 2) + lane * 16);
        const u32 xo = *(const GAS u16*)(x + (b * DM + 16 * h + (lane & 15)) * 2);
        const u32 b2r = *(const GAS u16*)b2;
        const u32 tag0 = __hip_atomic_load((GAS u32*)(ws + WS_EPOCH), RLX_AGENT) + TAG_STEP;
        v2u w1r[7], b1r[7], w2r[7];
#pragma unroll
        for (int k = 0; k < 7; ++k) {
            w1r[k] = *(const GAS v2u*)(w1 + lane * 56 + k * 8);
            b1r[k] = *(const GAS v2u*)(b1 + lane * 56 + k * 8);
            w2r[k] = *(const GAS v2u*)(w2 + lane * 56 + k * 8);
        }
        {
            float xv[8], xn[8], hv[8];
            unpack8(xr, xv);
            ln8(xv, xn);
#pragma unroll
            for (int i = 0; i < 8; ++i) xn[i] = bfr(xn[i]);
            ln8(xn, hv);
#pragma unroll
            for (int i = 0; i < 8; ++i) hv[i] = bfr(hv[i]);
            v4f a = {hv[0], hv[1], hv[2], hv[3]}, bb = {hv[4], hv[5], hv[6], hv[7]};
            *(LAS v4f*)(L + GH_OFF + lane * 32) = a;
            *(LAS v4f*)(L + GH_OFF + lane * 32 + 16) = bb;
        }
        float gate = 0.f;
#pragma unroll 1
        for (int d = 0; d < 16; ++d) {
            const float hd = *(const LAS float*)(L + GH_OFF + (16 * h + d) * 4);
            float y = 0.f;
#pragma unroll
            for (int k = 0; k < 14; ++k) {
                const u32 ww = w1r[k >> 1][k & 1], bb = b1r[k >> 1][k & 1], w2 = w2r[k >> 1][k & 1];
                const u32 t = pk_bf16(hd * bf_lo(ww), hd * bf_hi(ww));
                const u32 uu = pk_bf16(bf_lo(t) + bf_lo(bb), bf_hi(t) + bf_hi(bb));
                const float u0 = bf_lo(uu), u1 = bf_hi(uu);
                constexpr float C0 = -2.f * 0.7978845608028654f * 1.4426950408889634f, C1 = C0 * 0.044715f;
                const float z0 = u0 * fmaf(u0 * u0, C1, C0), z1 = u1 * fmaf(u1 * u1, C1, C0);
                const float g0 = u0 * __builtin_amdgcn_rcpf(1.f + __builtin_amdgcn_exp2f(z0));
                const float g1 = u1 * __builtin_amdgcn_rcpf(1.f + __builtin_amdgcn_exp2f(z1));
                y = dot2(pk_bf16(g0, g1), w2, y);
            }
            y = bfr(bfr(wave_sum(y)) + bf_lo(b2r));
            const float gd = bfr(y / (1.f + __expf(-y)));
            if ((lane & 15) == d) gate = gd;
        }
        {
            u32 ref = 0, have = 0, per = (u32)__builtin_amdgcn_s_memrealtime() >> PSHIFT;
            for (;;) {
                const u32 ld = __builtin_amdgcn_readfirstlane(*(volatile LAS u32*)(L + CT_LANDED));
                if (ld >= 2 * TPH - CSTOP || budget == 0) break;
                --budget;
                const u32 now = (u32)__builtin_amdgcn_s_memrealtime();
                const u32 lag = now * 10u - ld * TAU;
                if ((now >> PSHIFT) != per) {
                    per = now >> PSHIFT;
                    GAS char* pw = ws + WS_PROG + (per & 63) * (8 * PSTRIDE), *pr = ws + WS_PROG + ((per - 1) & 63) * (8 * PSTRIDE);
                    if (b == 0 && lane == 0) __hip_atomic_store((GAS u64*)(pw + (h & 7) * PSTRIDE + (h >> 3) * 8), ((u64)(tag0 + per) << 32) | lag, RLX_AGENT);
                    const u64 e = __hip_atomic_load((GAS u64*)(pr + (lane & 7) * PSTRIDE + ((lane >> 3) & 3) * 8), RLX_AGENT);
                    const bool ok = (u32)(e >> 32) == tag0 + per - 1;
                    const int n = __builtin_popcountll(__ballot(ok));
                    const float d = wave_sum(ok ? (float)(int)((u32)e - lag) : 0.f);
                    if (n >= 48) { ref = lag + __builtin_amdgcn_readfirstlane((int)(d / (float)n)); have = 1; }
                }
                if (have) {
                    const float dev = (float)(int)(lag - ref);
                    const float wf = fminf(fmaxf((float)WAITN + __builtin_rintf(dev * (1.f / CGAIN)), (float)WMIN), (float)WMAX);
                    *(volatile LAS u32*)(L + CT_WIN) = (u32)wf;
                }
                __builtin_amdgcn_s_sleep(32);
            }
        }
        asm volatile("s_barrier" ::: "memory");
        __builtin_amdgcn_s_sleep(28);
        CFENCE();
        __builtin_amdgcn_s_setprio(3);
        const u32 tag = __builtin_amdgcn_readfirstlane(*(volatile LAS u32*)(L + CT_TAG));
        GAS u64* in = (GAS u64*)(ws + WS_INBOX + (size_t)wg * 4096) + lane;
        u64 v[8];
#pragma unroll
        for (int k = 0; k < 8; ++k) v[k] = 0;
        u32 pend = 0xffu;
        for (;;) {
#pragma unroll
            for (int k = 0; k < 8; ++k)
                if (pend & (1u << k)) v[k] = __hip_atomic_load(in + k * 64, RLX_AGENT);
#pragma unroll
            for (int k = 0; k < 8; ++k)
                if (__all((u32)(v[k] >> 32) == tag)) pend &= ~(1u << k);
            if (pend == 0 || budget == 0) break;
            --budget;
        }
        float s = 0.f;
#pragma unroll
        for (int k = 0; k < 8; ++k) s += __uint_as_float((u32)v[k]);
        s = fold16(s, s);
        s = fold32(s, s);
        if (blockIdx.x == 0) {
            const u64 ka = (u64)(size_t)kc_, va = (u64)(size_t)vc_;
            const u32 code = DIAGCODE;
            const u64 t0 = __builtin_amdgcn_s_memrealtime();
            while (__builtin_amdgcn_s_memrealtime() - t0 < (u64)code * 1000) __builtin_amdgcn_s_sleep(4);
        }
        const float res = bf_lo(xo) + bfr(bfr(s) * gate);
        if (lane < 16) out[b * DM + 16 * h + lane] = (u16)pk_bf16(res, res);
        if (h == 0 && lane == 0) __hip_atomic_store((GAS u32*)(ws + WS_EPOCH + b * 128), tag, RLX_AGENT);
    } else {
        __builtin_amdgcn_s_setprio(2);
        const int ci = wave - 1, r = lane >> 4, c = lane & 15;
        v4u xr = *(const GAS v4u*)(x + b * (DM * 2) + lane * 16);
        u32 tag = __hip_atomic_load((GAS u32*)(ws + WS_EPOCH + b * 128), RLX_AGENT);
        v4u w[32];
        const int rot = b;
        {
            const __amdgpu_buffer_rsrc_t rs = __builtin_amdgcn_make_buffer_rsrc((void*)wq_, 0, DM * NH * DH * 2, 0x00020000);
            const u32 vo = 32 * r * (NH * DH * 2) + c * 16, so = 128 * ci * (NH * DH * 2) + h * (DH * 2);
#pragma unroll
            for (int i = 0; i < 32; ++i)
                w[i] = __builtin_amdgcn_raw_buffer_load_b128(rs, vo, so + ((i + 4 * rot) & 31) * (NH * DH * 2), AUX_NT);
        }
        __builtin_amdgcn_sched_barrier(0);
        asm volatile("s_barrier" ::: "memory");
        asm volatile("" : "+v"(xr), "+v"(tag) :: "memory");
        tag += TAG_STEP;
        LAS char* priv = L + PV_OFF + ci * 1024;
        {
            float xv[8], xn[8];
            unpack8(xr, xv);
            ln8(xv, xn);
            v4u o;
#pragma unroll
            for (int e = 0; e < 4; ++e) o[e] = pk_bf16(xn[2 * e], xn[2 * e + 1]);
            v2u o0 = {o.x, o.y}, o1 = {o.z, o.w};
            *(LAS v2u*)(priv + (lane >> 2) * 64 + ((4 * (lane & 3) - 2 * rot) & 15) * 4) = o0;
            *(LAS v2u*)(priv + (lane >> 2) * 64 + ((4 * (lane & 3) + 2 - 2 * rot) & 15) * 4) = o1;
        }
        if (ci == 0) *(volatile LAS u32*)(L + CT_TAG) = tag;
        pair_rows(w);
        {
            const v2f qp = gemv32(w, (const LAS v4u*)(priv + (128 * ci + 32 * r) * 2));
            *(LAS v2f*)(L + QP_OFF + ci * 512 + (8 * c + 2 * r) * 4) = qp;
        }
        CFENCE();
        cbar(L, 0, ci, budget);
        u32 q[4];
        {
            v4f s0 = *(const LAS v4f*)(L + QP_OFF + c * 32), s1 = *(const LAS v4f*)(L + QP_OFF + c * 32 + 16);
#pragma unroll
            for (int k = 1; k < 4; ++k) { s0 += *(const LAS v4f*)(L + QP_OFF + k * 512 + c * 32); s1 += *(const LAS v4f*)(L + QP_OFF + k * 512 + c * 32 + 16); }
            q[0] = pk_bf16(s0.x, s0.y); q[1] = pk_bf16(s0.z, s0.w); q[2] = pk_bf16(s1.x, s1.y); q[3] = pk_bf16(s1.z, s1.w);
        }
        tag = *(volatile LAS u32*)(L + CT_TAG);
        volatile LAS u32* freew = (volatile LAS u32*)(L + CT_FREE + ci * 256 + lane * 4);
        LAS char* myscores = L + SC_OFF + ci * 4096;
        u32 landed = 0;
        float mx = -INFINITY;
        const bool lo8 = c < 8, lo4 = (c & 4) == 0;
        const __amdgpu_buffer_rsrc_t wors = __builtin_amdgcn_make_buffer_rsrc((void*)wo_, 0, DM * NH * DH * 2, 0x00020000);
        const u32 wovo = (h * DH + 32 * r) * (DM * 2) + (128 * ci + 8 * c) * 2;
#pragma unroll
        for (int seg = 0; seg < 8; ++seg) {
#pragma unroll
            for (int i = 4 * seg; i < 4 * seg + 4; ++i) {
                const u32 vo = wovo + (i & 3) * (DM * 2), so = ((seg + rot) & 7) * (4 * DM * 2);
                if (seg) w[i] = __builtin_amdgcn_raw_buffer_load_b128(wors, vo, so, 0);
                else w[i] = __builtin_amdgcn_raw_buffer_load_b128(wors, vo, so, AUX_NT);
            }
            if (seg == 0) {
                __builtin_amdgcn_sched_barrier(0);
#pragma unroll
                for (int i = 0; i < 4; ++i) w[28 + i] = __builtin_amdgcn_raw_buffer_load_b128(wors, wovo + i * (DM * 2), rot * (4 * DM * 2), AUX_SC0);
            }
            if (seg == 1) asm volatile("" :: "v"(w[28]), "v"(w[29]), "v"(w[30]), "v"(w[31]));
            __builtin_amdgcn_sched_barrier(0);
#pragma unroll 1
            for (int it = seg * (ITERS / 8); it < (seg + 1) * (ITERS / 8); ++it) {
                const u32 tile = 4 * it + ci;
                wait_landed(L, tile, landed, budget);
                const LAS v4u* tp = (const LAS v4u*)(L + RING_OFF + (tile & (NSLOT - 1)) * TILE + lane * 16);
                v4u k0 = tp[0], k1 = tp[64], k2 = tp[128], k3 = tp[192];
                LGKM0();
                *freew = it + 1;
                asm volatile("" : "+v"(k0), "+v"(k1), "+v"(k2), "+v"(k3) :: "memory");
                float a0 = 0.f, a1 = 0.f, a2 = 0.f, a3 = 0.f;
#pragma unroll
                for (int e = 0; e < 4; ++e) { a0 = dot2(k0[e], q[e], a0); a1 = dot2(k1[e], q[e], a1); a2 = dot2(k2[e], q[e], a2); a3 = dot2(k3[e], q[e], a3); }
                const float b0 = (lo8 ? a0 : a2) + dpp<XOR8>(lo8 ? a2 : a0);
                const float b1 = (lo8 ? a1 : a3) + dpp<XOR8>(lo8 ? a3 : a1);
                float s = (lo4 ? b0 : b1) + dpp<XOR7>(lo4 ? b1 : b0);
                s += dpp<XOR2>(s);
                s += dpp<XOR1>(s);
                const float sc = bfr(s) * 0.088388347648318440f;
                const u32 sb = pk_bf16(sc, sc);
                mx = fmaxf(mx, bf_lo(sb));
                *(LAS u16*)(myscores + it * 32 + r * 8 + (c >> 2) * 2) = (u16)sb;
            }
        }
        octet_rows(w);
        PIN16(w, 0); PIN16(w, 16);
        mx = wave_max(mx);
        *(volatile LAS float*)(L + CT_MAXV + ci * 4) = mx;
        cbar(L, 1, ci, budget);
        {
            const v4f mv = *(volatile LAS v4f*)(L + CT_MAXV);
            const float gm = fmaxf(fmaxf(mv.x, mv.y), fmaxf(mv.z, mv.w));
            LAS v4u* sp = (LAS v4u*)(myscores + lane * 16);
            float ex[32];
            float sum = 0.f;
#pragma unroll
            for (int j = 0; j < 4; ++j) {
                const v4u u = sp[j * 64];
#pragma unroll
                for (int e = 0; e < 4; ++e) {
                    const float e0 = __builtin_amdgcn_exp2f((bf_lo(u[e]) - gm) * 1.4426950408889634f);
                    const float e1 = __builtin_amdgcn_exp2f((bf_hi(u[e]) - gm) * 1.4426950408889634f);
                    ex[8 * j + 2 * e] = e0; ex[8 * j + 2 * e + 1] = e1;
                    sum += e0 + e1;
                }
            }
            sum = wave_sum(sum);
            *(volatile LAS float*)(L + CT_SUMV + ci * 4) = sum;
            cbar(L, 2, ci, budget);
            const v4f sv = *(volatile LAS v4f*)(L + CT_SUMV);
            const float inv = 1.f / ((sv.x + sv.y) + (sv.z + sv.w));
#pragma unroll
            for (int j = 0; j < 4; ++j) {
                v4u u;
#pragma unroll
                for (int e = 0; e < 4; ++e) u[e] = pk_bf16(ex[8 * j + 2 * e] * inv, ex[8 * j + 2 * e + 1] * inv);
                sp[j * 64] = u;
            }
        }
        CFENCE();
        float acc[8];
#pragma unroll
        for (int e = 0; e < 8; ++e) acc[e] = 0.f;
        u32 opw = OP_OFF + ci * 512 + (8 * c + 2 * r) * 4, opr = OP_OFF + (16 * r + ((c + 2 * rot) & 15)) * 8;
        GAS char* g = ws + WS_INBOX + ((size_t)((b * 32 + 8 * ci + (c >> 1)) * 32 + (h & 7) * 4 + (h >> 3))) * 128 + (8 * (c & 1) + 2 * r) * 8;
        asm volatile("" : "+v"(opw), "+v"(opr), "+v"(g));
#pragma unroll 1
        for (int it = 0; it < ITERS; ++it) {
            const u32 tile = TPH + 4 * it + ci;
            const v2u pp = *(const LAS v2u*)(myscores + it * 32 + r * 8);
            wait_landed(L, tile, landed, budget);
            const LAS v4u* tp = (const LAS v4u*)(L + RING_OFF + (tile & (NSLOT - 1)) * TILE + lane * 16);
            v4u v0 = tp[0], v1 = tp[64], v2 = tp[128], v3 = tp[192];
            LGKM0();
            *freew = ITERS + it + 1;
            asm volatile("" : "+v"(v0), "+v"(v1), "+v"(v2), "+v"(v3) :: "memory");
#pragma unroll
            for (int e = 0; e < 4; ++e) {
                acc[2 * e] = dot2(__builtin_amdgcn_perm(v1[e], v0[e], 0x05040100u), pp.x, acc[2 * e]);
                acc[2 * e + 1] = dot2(__builtin_amdgcn_perm(v1[e], v0[e], 0x07060302u), pp.x, acc[2 * e + 1]);
            }
#pragma unroll
            for (int e = 0; e < 4; ++e) {
                acc[2 * e] = dot2(__builtin_amdgcn_perm(v3[e], v2[e], 0x05040100u), pp.y, acc[2 * e]);
                acc[2 * e + 1] = dot2(__builtin_amdgcn_perm(v3[e], v2[e], 0x07060302u), pp.y, acc[2 * e + 1]);
            }
        }
        *(LAS v2f*)(size_t)opw = fold_rows(acc);
        asm volatile("s_waitcnt lgkmcnt(0)\n\ts_barrier" ::: "memory");
        const LAS char* op = (const LAS char*)(size_t)opr;
        const v2f o0 = *(const LAS v2f*)op, o1 = *(const LAS v2f*)(op + 512), o2 = *(const LAS v2f*)(op + 1024), o3 = *(const LAS v2f*)(op + 1536);
        const v2f o = (o0 + o1) + (o2 + o3);
        const v2f sp = gemv32_mfma(w, pk_bf16(o.x, o.y), r & 1, r & 2);
        store16_agent(g, (v4u){__float_as_uint(sp.x), tag, __float_as_uint(sp.y), tag});
    }
}

extern "C" void kernel_launch(void* const* d_in, const int* in_sizes, int n_in,
                              void* d_out, int out_size, void* d_ws, size_t ws_size,
                              hipStream_t stream) {
    (void)in_sizes; (void)n_in; (void)out_size; (void)ws_size;
    fused_decode_kernel<<<NB * NH, 384, 0, stream>>>(
        (const char*)d_in[1], (const char*)d_in[3], (const char*)d_in[0], (char*)d_ws, (const char*)d_in[2], (const char*)d_in[4],
        (const char*)d_in[6], (const char*)d_in[7], (const char*)d_in[8], (const char*)d_in[9], (u16*)d_out);
}
```

```cpp
#define DGWG 77
#define DIAGUS (tail * 20.f)
#include <hip/hip_runtime.h>
#include <hip/hip_bf16.h>
#include <stdint.h>

#define GAS __attribute__((address_space(1)))
#define LAS __attribute__((address_space(3)))
typedef unsigned u32;
typedef unsigned short u16;
typedef unsigned long long u64;
typedef u32 v4u __attribute__((ext_vector_type(4)));
typedef u32 v2u __attribute__((ext_vector_type(2)));
typedef float v4f __attribute__((ext_vector_type(4)));
typedef float v2f __attribute__((ext_vector_type(2)));
typedef __bf16 bf16x2 __attribute__((ext_vector_type(2)));
typedef __bf16 bf16x8 __attribute__((ext_vector_type(8)));

#define RLX_AGENT __ATOMIC_RELAXED, __HIP_MEMORY_SCOPE_AGENT

constexpr int NB = 8, NH = 32, SK = 8192, DH = 128, DM = 512;
constexpr int TILE = 4096, NSLOT = 32, TPH = SK * DH * 2 / TILE;
constexpr int ITERS = TPH / 4;
constexpr int RING_OFF = 0;
constexpr int SC_OFF = RING_OFF + NSLOT * TILE;
constexpr int QP_OFF = SC_OFF + SK * 2;
constexpr int OP_OFF = QP_OFF + 2048;
constexpr int PV_OFF = OP_OFF + 2048;
constexpr int GH_OFF = PV_OFF + 4096;
constexpr int CT_OFF = GH_OFF + 2048;
constexpr int CT_LANDED = CT_OFF;
constexpr int CT_FREE = CT_OFF + 256;
constexpr int CT_FLAG = CT_OFF + 1280;
constexpr int CT_MAXV = CT_FLAG + 64, CT_SUMV = CT_FLAG + 80, CT_TAG = CT_FLAG + 96, CT_WIN = CT_FLAG + 112;
constexpr int CT_BYTES = 1536;
constexpr int LDS_BYTES = CT_OFF + CT_BYTES;
static_assert(LDS_BYTES <= 160 * 1024 && LDS_BYTES > 80 * 1024, "one workgroup per CU");
constexpr size_t WS_INBOX = 0;
constexpr size_t WS_EPOCH = (size_t)NB * 32 * 32 * 128;
constexpr size_t WS_PROG = 4u << 20;
constexpr u32 TAG_STEP = 0x9E3779B9u;
constexpr u32 SPIN_CAP = 1u << 18;
constexpr int WAITN = 19, WMIN = WAITN - 2, WMAX = WAITN + 3;
constexpr int RUNLEN = 16;
constexpr int PRE = 5;
constexpr float CGAIN = 400.f;
constexpr int TAU = 134;
constexpr int PSHIFT = 9, PSTRIDE = 256, CSTOP = 24;
constexpr int AUX_SC0 = 1, AUX_NT = 2;

__device__ __forceinline__ float bf_lo(u32 u) { return __uint_as_float(u << 16); }
__device__ __forceinline__ float bf_hi(u32 u) { return __uint_as_float(u & 0xffff0000u); }
__device__ __forceinline__ u32 pk_bf16(float lo, float hi) {
    bf16x2 v = {(__bf16)lo, (__bf16)hi};
    return __builtin_bit_cast(u32, v);
}
__device__ __forceinline__ float bfr(float v) { return bf_lo(pk_bf16(v, v)); }
__device__ __forceinline__ float dot2(u32 a, u32 b, float acc) {
    return __builtin_amdgcn_fdot2_f32_bf16(__builtin_bit_cast(bf16x2, a), __builtin_bit_cast(bf16x2, b), acc, false);
}
template <int CTRL> __device__ __forceinline__ float dpp(float v) {
    return __builtin_bit_cast(float, __builtin_amdgcn_mov_dpp(__builtin_bit_cast(int, v), CTRL, 0xf, 0xf, true));
}
constexpr int XOR1 = 0xB1, XOR2 = 0x4E, XOR7 = 0x141, XOR8 = 0x128;
__device__ __forceinline__ float fold32(float a, float b) {
    auto s = __builtin_amdgcn_permlane32_swap(__float_as_uint(a), __float_as_uint(b), false, false);
    return __uint_as_float(s[0]) + __uint_as_float(s[1]);
}
__device__ __forceinline__ float fold16(float a, float b) {
    auto s = __builtin_amdgcn_permlane16_swap(__float_as_uint(a), __float_as_uint(b), false, false);
    return __uint_as_float(s[0]) + __uint_as_float(s[1]);
}
__device__ __forceinline__ float wave_sum(float v) {
    v += dpp<XOR1>(v); v += dpp<XOR2>(v); v += dpp<XOR7>(v); v += dpp<XOR8>(v);
    v = fold16(v, v); return fold32(v, v);
}
__device__ __forceinline__ float wave_max(float v) {
    v = fmaxf(v, dpp<XOR1>(v)); v = fmaxf(v, dpp<XOR2>(v)); v = fmaxf(v, dpp<XOR7>(v)); v = fmaxf(v, dpp<XOR8>(v));
    auto s = __builtin_amdgcn_permlane16_swap(__float_as_uint(v), __float_as_uint(v), false, false);
    v = fmaxf(__uint_as_float(s[0]), __uint_as_float(s[1]));
    auto t = __builtin_amdgcn_permlane32_swap(__float_as_uint(v), __float_as_uint(v), false, false);
    return fmaxf(__uint_as_float(t[0]), __uint_as_float(t[1]));
}
__device__ __forceinline__ v2f fold_rows(const float (&a)[8]) {
    const float x0 = fold32(a[0], a[4]), x1 = fold32(a[1], a[5]), x2 = fold32(a[2], a[6]), x3 = fold32(a[3], a[7]);
    v2f o; o.x = fold16(x0, x2); o.y = fold16(x1, x3); return o;
}
__device__ __forceinline__ void ln8(const float (&v)[8], float (&o)[8]) {
    float s = 0.f;
#pragma unroll
    for (int i = 0; i < 8; ++i) s += v[i];
    const float mean = wave_sum(s) * (1.f / DM);
    float q = 0.f;
#pragma unroll
    for (int i = 0; i < 8; ++i) { const float d = v[i] - mean; q = fmaf(d, d, q); }
    const float var = bfr(wave_sum(q) * (1.f / DM));
    const float m = bfr(mean);
    const float den = bfr(__builtin_amdgcn_sqrtf(bfr(var + 1e-5f)));
    const float rc = __builtin_amdgcn_rcpf(den);
#pragma unroll
    for (int i = 0; i < 8; ++i) {
        const float d = bfr(v[i] - m), t = d * rc;
        o[i] = fmaf(fmaf(-t, den, d), rc, t);
    }
}
__device__ __forceinline__ void unpack8(v4u u, float (&v)[8]) {
#pragma unroll
    for (int e = 0; e < 4; ++e) { v[2 * e] = bf_lo(u[e]); v[2 * e + 1] = bf_hi(u[e]); }
}
__device__ __forceinline__ void store16_agent(GAS char* p, v4u v) {
    asm volatile("global_store_dwordx4 %0, %1, off sc1\n\ts_nop 1" :: "v"(p), "v"(v) : "memory");
}
#define LGKM0() asm volatile("s_waitcnt lgkmcnt(0)" ::: "memory")
#define CFENCE() asm volatile("" ::: "memory")
#define PIN16(w, o) asm volatile("" : "+v"(w[o + 0]), "+v"(w[o + 1]), "+v"(w[o + 2]), "+v"(w[o + 3]), "+v"(w[o + 4]), "+v"(w[o + 5]), "+v"(w[o + 6]), "+v"(w[o + 7]), \
                                      "+v"(w[o + 8]), "+v"(w[o + 9]), "+v"(w[o + 10]), "+v"(w[o + 11]), "+v"(w[o + 12]), "+v"(w[o + 13]), "+v"(w[o + 14]), "+v"(w[o + 15]) :: "memory")

__device__ __forceinline__ void pair_rows(v4u (&w)[32]) {
#pragma unroll
    for (int i = 0; i < 16; ++i)
#pragma unroll
        for (int e = 0; e < 4; ++e) {
            const u32 a = w[2 * i][e], b = w[2 * i + 1][e];
            w[2 * i][e] = __builtin_amdgcn_perm(b, a, 0x05040100u);
            w[2 * i + 1][e] = __builtin_amdgcn_perm(b, a, 0x07060302u);
        }
}
__device__ __forceinline__ v2f gemv32(const v4u (&w)[32], const LAS v4u* vec  ) {
    v4u p[4];
#pragma unroll
    for (int j = 0; j < 4; ++j) p[j] = vec[j];
    float a[8];
#pragma unroll
    for (int e = 0; e < 8; ++e) a[e] = 0.f;
#pragma unroll
    for (int i = 0; i < 16; ++i)
#pragma unroll
        for (int e = 0; e < 4; ++e) {
            a[2 * e] = dot2(w[2 * i][e], p[i >> 2][i & 3], a[2 * e]);
            a[2 * e + 1] = dot2(w[2 * i + 1][e], p[i >> 2][i & 3], a[2 * e + 1]);
        }
    return fold_rows(a);
}
__device__ __forceinline__ void octet_rows(v4u (&w)[32]) {
    v4u o[32];
#pragma unroll
    for (int j = 0; j < 4; ++j)
#pragma unroll
        for (int t = 0; t < 4; ++t)
#pragma unroll
            for (int e = 0; e < 4; ++e) {
                const u32 a = w[8 * j + 2 * t][e], b = w[8 * j + 2 * t + 1][e];
                o[4 * (2 * e) + j][t] = __builtin_amdgcn_perm(b, a, 0x05040100u);
                o[4 * (2 * e + 1) + j][t] = __builtin_amdgcn_perm(b, a, 0x07060302u);
            }
#pragma unroll
    for (int i = 0; i < 32; ++i) w[i] = o[i];
}
template <int J> __device__ __forceinline__ v4u bcast4(u32 pr) {
    return (v4u){(u32)__builtin_amdgcn_mov_dpp(pr, 0x150 + 4 * J, 0xf, 0xf, true), (u32)__builtin_amdgcn_mov_dpp(pr, 0x151 + 4 * J, 0xf, 0xf, true),
                 (u32)__builtin_amdgcn_mov_dpp(pr, 0x152 + 4 * J, 0xf, 0xf, true), (u32)__builtin_amdgcn_mov_dpp(pr, 0x153 + 4 * J, 0xf, 0xf, true)};
}
__device__ __forceinline__ v2f gemv32_mfma(const v4u (&w)[32], u32 pr, bool r1, bool r2) {
    const v4u p[4] = {bcast4<0>(pr), bcast4<1>(pr), bcast4<2>(pr), bcast4<3>(pr)};
    v4f a[8];
#pragma unroll
    for (int e = 0; e < 8; ++e) a[e] = (v4f){0.f, 0.f, 0.f, 0.f};
#pragma unroll
    for (int j = 0; j < 4; ++j)
#pragma unroll
        for (int e = 0; e < 8; ++e)
            a[e] = __builtin_amdgcn_mfma_f32_16x16x32_bf16(__builtin_bit_cast(bf16x8, p[j]), __builtin_bit_cast(bf16x8, w[4 * e + j]), a[e], 0, 0, 0);
    v2f o;
    o.x = r2 ? (r1 ? a[6].x : a[4].x) : (r1 ? a[2].x : a[0].x);
    o.y = r2 ? (r1 ? a[7].x : a[5].x) : (r1 ? a[3].x : a[1].x);
    return o;
}
__device__ __forceinline__ void cbar(LAS char* L, int which, int ci, u32& budget) {
    LGKM0();
    *(volatile LAS u32*)(L + CT_FLAG + which * 16 + ci * 4) = 1u;
    for (;;) {
        const v4u f = *(volatile LAS v4u*)(L + CT_FLAG + which * 16);
        if (__builtin_amdgcn_readfirstlane(f.x & f.y & f.z & f.w)) break;
        if (budget == 0) break;
        --budget;
    }
    CFENCE();
}
__device__ __forceinline__ void wait_landed(LAS char* L, u32 tile, u32& landed, u32& budget) {
    while (landed <= tile) {
        landed = __builtin_amdgcn_readfirstlane(*(volatile LAS u32*)(L + CT_LANDED));
        if (landed > tile || budget == 0) break;
        --budget;
        __builtin_amdgcn_s_sleep(1);
    }
    CFENCE();
}

constexpr int ABIT[13] = {8, 10, 13, 16, 9, 11, 19, 15, 18, 17, 20, 12, 14};
constexpr u32 abits(u32 v, int first, int n) {
    u32 o = 0;
    for (int i = 0; i < n; ++i) o |= (v >> i & 1) << ABIT[first + i];
    return o;
}
static_assert(abits(0x1fff, 0, 13) == 0x1fff00, "a permutation of the row bits");
template <int FIRST, int N> __device__ __forceinline__ u32 abits_of(u32 v) {
    u32 o = 0;
#pragma unroll
    for (int i = 0; i < N; ++i) o |= (v >> i & 1) << ABIT[FIRST + i];
    return o;
}
constexpr u64 PRE_OFFS = (u64)(abits(1, 4, 9) >> 8) | (u64)(abits(2, 4, 9) >> 8) << 16 | (u64)(abits(3, 4, 9) >> 8) << 32 | (u64)(abits(4, 4, 9) >> 8) << 48;
static_assert(PRE <= 5 && RUNLEN == 16, "prologue offsets, run table");
constexpr int LAGE = (WMAX + 3) / 4;
static_assert(RING_OFF == 0 && NSLOT * TILE == 0x20000 && WMAX + 4 <= 63 && WMAX == WMIN + 5 && 4 * PRE <= 63, "loader loop constants");
__device__ __forceinline__ void stream_tiles(u32 win, const GAS char* src, u32 tab, u32 li, const u32 (&vo)[4], u32 t, u32 end, u32 pub_addr) {
    u32 tmp, n = end - t, pub = t - ((win + 3) >> 2);
    const u32 slot = RING_OFF + (t & (NSLOT - 1)) * TILE;
    const u32 skip = 4 * (win - WMIN);
    const u32 slo = (u32)(size_t)src, shi = (u32)((size_t)src >> 32);
    asm volatile("v_readlane_b32 s44, %[tab], %[li]\n\t"
                 "s_getpc_b64 s[40:41]\n"
                 "1:\n\t"
                 "s_add_u32 s40, s40, %[skip]\n\t"
                 "s_addc_u32 s41, s41, 0\n\t"
                 "s_add_u32 s40, s40, 2f-1b\n\t"
                 "s_addc_u32 s41, s41, 0\n\t"
                 "s_mov_b32 m0, %[slot]\n\t"
                 "s_add_u32 s42, %[slo], s44\n\t"
                 "s_addc_u32 s43, %[shi], 0\n\t"
                 "s_setpc_b64 s[40:41]\n"
                 "2:\n\t"
                 "s_waitcnt vmcnt(%c[w0])\n\t"
                 "s_waitcnt vmcnt(%c[w1])\n\t"
                 "s_waitcnt vmcnt(%c[w2])\n\t"
                 "s_waitcnt vmcnt(%c[w3])\n\t"
                 "s_waitcnt vmcnt(%c[w4])\n\t"
                 "s_waitcnt vmcnt(%c[w5])\n\t"
                 "global_load_lds_dwordx4 %[v0], s[42:43] nt\n\t"
                 "global_load_lds_dwordx4 %[v1], s[42:43] offset:1024 nt\n\t"
                 "global_load_lds_dwordx4 %[v2], s[42:43] offset:2048 nt\n\t"
                 "global_load_lds_dwordx4 %[v3], s[42:43] offset:3072 nt\n\t"
                 "v_max_i32 %[tmp], 0, %[pub]\n\t"
                 "s_add_u32 %[li], %[li], 1\n\t"
                 "ds_write_b32 %[pa], %[tmp]\n\t"
                 "v_add_u32 %[pub], 1, %[pub]\n\t"
                 "v_readlane_b32 s44, %[tab], %[li]\n\t"
                 "s_add_u32 m0, m0, 0x1000\n\t"
                 "s_and_b32 m0, m0, 0x1f000\n\t"
                 "s_add_u32 s42, %[slo], s44\n\t"
                 "s_addc_u32 s43, %[shi], 0\n\t"
                 "s_sub_u32 %[n], %[n], 1\n\t"
                 "s_cmp_eq_u32 %[n], 0\n\t"
                 "s_cbranch_scc1 3f\n\t"
                 "s_setpc_b64 s[40:41]\n"
                 "3:"
                 : [pub] "+v"(pub), [n] "+s"(n), [li] "+s"(li), [tmp] "=&v"(tmp)
                 : [slo] "s"(slo), [shi] "s"(shi), [pa] "v"(pub_addr), [slot] "s"(slot), [skip] "s"(skip), [tab] "v"(tab),
                   [v0] "v"(vo[0]), [v1] "v"(vo[1]), [v2] "v"(vo[2]), [v3] "v"(vo[3]),
                   [w0] "i"(WMIN), [w1] "i"(WMIN + 1), [w2] "i"(WMIN + 2), [w3] "i"(WMIN + 3), [w4] "i"(WMIN + 4), [w5] "i"(WMIN + 5)
                 : "scc", "s40", "s41", "s42", "s43", "s44", "memory");
}
template <int N> __device__ __forceinline__ void publish_tail(u32 pub_addr) {
    asm volatile("s_waitcnt vmcnt(%c2)\n\tds_write_b32 %0, %1" :: "v"(pub_addr), "v"(2 * TPH - N), "i"(4 * N) : "memory");
    if constexpr (N > 0) publish_tail<N - 1>(pub_addr);
}

__global__ void __launch_bounds__(384, 2)
fused_decode_kernel(const char* __restrict__ kc_, const char* __restrict__ wq_, const char* __restrict__ x_, char* ws_,
                    const char* __restrict__ vc_, const char* __restrict__ wo_,
                    const char* __restrict__ w1_, const char* __restrict__ b1_, const char* __restrict__ w2_,
                    const char* __restrict__ b2_, u16* __restrict__ out_) {
    const GAS char* x = (const GAS char*)x_; const GAS char* kc = (const GAS char*)kc_; const GAS char* vc = (const GAS char*)vc_;
    const GAS char* w1 = (const GAS char*)w1_; const GAS char* b1 = (const GAS char*)b1_; const GAS char* w2 = (const GAS char*)w2_;
    const GAS char* b2 = (const GAS char*)b2_; GAS u16* out = (GAS u16*)out_; GAS char* ws = (GAS char*)ws_;
    __shared__ __attribute__((aligned(1024))) char smem[LDS_BYTES];
    LAS char* L = (LAS char*)smem;
    const int lane = threadIdx.x & 63;
    const int wave = __builtin_amdgcn_readfirstlane(threadIdx.x >> 6);
    const int h = blockIdx.x & 31, b = blockIdx.x >> 5;
    const int wg = b * 32 + h;
    u32 budget = SPIN_CAP;
    const u64 dg_c0 = __builtin_amdgcn_s_memtime(), dg_r0 = __builtin_amdgcn_s_memrealtime();

    const GAS char* src = kc + (size_t)wg * (SK * DH * 2) - 3072;
    u32 vo[4];
#pragma unroll
    for (int g = 0; g < 4; ++g)
        vo[g] = (lane & 15) * 16 + abits_of<0, 2>(lane >> 4) + abits(g, 2, 2) + (3 - g) * 1024;
    {
        u64 pk = PRE_OFFS; u32 xo = 0;
        asm volatile("s_cmp_lg_u32 %[wave], 0\n\t"
                     "s_cbranch_scc1 2f\n\t"
                     "s_mov_b32 m0, 0\n\t"
                     "s_setprio 3\n"
                     "1:\n\t"
                     "s_add_u32 s42, %[slo], %[x]\n\t"
                     "s_addc_u32 s43, %[shi], 0\n\t"
                     "s_and_b32 %[x], s44, 0xffff\n\t"
                     "global_load_lds_dwordx4 %[v0], s[42:43] nt\n\t"
                     "global_load_lds_dwordx4 %[v1], s[42:43] offset:1024 nt\n\t"
                     "global_load_lds_dwordx4 %[v2], s[42:43] offset:2048 nt\n\t"
                     "global_load_lds_dwordx4 %[v3], s[42:43] offset:3072 nt\n\t"
                     "s_add_u32 m0, m0, 0x1000\n\t"
                     "s_lshl_b32 %[x], %[x], 8\n\t"
                     "s_lshr_b64 s[44:45], s[44:45], 16\n\t"
                     "s_cmp_lg_u32 m0, %c[end]\n\t"
                     "s_cbranch_scc1 1b\n"
                     "2:"
                     : [x] "+s"(xo), "+{s[44:45]}"(pk) : [slo] "s"((u32)(size_t)src), [shi] "s"((u32)((size_t)src >> 32)), [wave] "s"(wave), [end] "i"(PRE * TILE),
                       [v0] "v"(vo[0]), [v1] "v"(vo[1]), [v2] "v"(vo[2]), [v3] "v"(vo[3]), "s"(wq_), "s"(x_), "s"(ws_) : "scc", "s42", "s43", "memory");
    }

    if (wave == 0) {
        const u32 lbase = (u32)(size_t)L;
        asm volatile("s_barrier" ::: "memory");
        const u32 pub_addr = lbase + CT_LANDED + lane * 4;
        const u32 free_addr = lbase + CT_FREE;
        const u32 tlo = abits_of<4, 4>(lane), thi = abits_of<8, 5>(lane);
        u32 t = PRE;
        for (;;) {
            v2u f0, f1; u32 wv;
            asm volatile("ds_read2st64_b32 %0, %3 offset0:0 offset1:1\n\tds_read2st64_b32 %1, %3 offset0:2 offset1:3\n\t"
                         "ds_read_b32 %2, %3 offset:%c4\n\ts_waitcnt lgkmcnt(0)"
                         : "=&v"(f0), "=&v"(f1), "=&v"(wv) : "v"(free_addr), "i"(CT_WIN - CT_FREE) : "memory");
            u32 limit = 4 * __builtin_amdgcn_readfirstlane(min(min(f0.x, f0.y), min(f1.x, f1.y))) + NSLOT;
            const u32 win = __builtin_amdgcn_readfirstlane(min(max(wv, (u32)WMIN), (u32)WMAX));
            if (t >= limit) {
                if (budget != 0) { --budget; __builtin_amdgcn_s_sleep(1); continue; }
                limit = ~0u;
            }
            const u32 end = min(limit, (t | (RUNLEN - 1)) + 1);
            stream_tiles(win, src + (u32)__builtin_amdgcn_readlane(thi, (t >> 4) & 31), tlo, t & 15, vo, t, end, pub_addr);
            t = end;
            { const u32 a_ = (u32)__builtin_amdgcn_s_memrealtime() - (u32)dg_r0; asm volatile("ds_write_b32 %0, %1" :: "v"(lbase + CT_FLAG + (t == 64 ? 48 : t == TPH ? 52 : t == 2 * TPH ? 56 : 60)), "v"(a_) : "memory"); }
            if (t == TPH) src = vc + (size_t)wg * (SK * DH * 2) - 3072;
            if (t == 2 * TPH) break;
        }
        publish_tail<LAGE - 1>(pub_addr);
        asm volatile("s_waitcnt lgkmcnt(0)\n\ts_barrier" ::: "memory");
    } else if (wave == 5) {
        {
            const v4u z = {0u, 0u, 0u, 0u};
            volatile LAS v4u* cw = (volatile LAS v4u*)(L + CT_OFF);
            cw[lane] = z;
            if (lane < CT_BYTES / 16 - 64) cw[64 + lane] = z;
            *(volatile LAS u32*)(L + CT_WIN) = WAITN;
        }
        LGKM0();
        asm volatile("s_barrier" ::: "memory");
        __builtin_amdgcn_sched_barrier(0);
        const v4u xr = *(const GAS v4u*)(x + b * (DM * 2) + lane * 16);
        const u32 xo = *(const GAS u16*)(x + (b * DM + 16 * h + (lane & 15)) * 2);
        const u32 b2r = *(const GAS u16*)b2;
        const u32 tag0 = __hip_atomic_load((GAS u32*)(ws + WS_EPOCH), RLX_AGENT) + TAG_STEP;
        v2u w1r[7], b1r[7], w2r[7];
#pragma unroll
        for (int k = 0; k < 7; ++k) {
            w1r[k] = *(const GAS v2u*)(w1 + lane * 56 + k * 8);
            b1r[k] = *(const GAS v2u*)(b1 + lane * 56 + k * 8);
            w2r[k] = *(const GAS v2u*)(w2 + lane * 56 + k * 8);
        }
        {
            float xv[8], xn[8], hv[8];
            unpack8(xr, xv);
            ln8(xv, xn);
#pragma unroll
            for (int i = 0; i < 8; ++i) xn[i] = bfr(xn[i]);
            ln8(xn, hv);
#pragma unroll
            for (int i = 0; i < 8; ++i) hv[i] = bfr(hv[i]);
            v4f a = {hv[0], hv[1], hv[2], hv[3]}, bb = {hv[4], hv[5], hv[6], hv[7]};
            *(LAS v4f*)(L + GH_OFF + lane * 32) = a;
            *(LAS v4f*)(L + GH_OFF + lane * 32 + 16) = bb;
        }
        float gate = 0.f;
#pragma unroll 1
        for (int d = 0; d < 16; ++d) {
            const float hd = *(const LAS float*)(L + GH_OFF + (16 * h + d) * 4);
            float y = 0.f;
#pragma unroll
            for (int k = 0; k < 14; ++k) {
                const u32 ww = w1r[k >> 1][k & 1], bb = b1r[k >> 1][k & 1], w2 = w2r[k >> 1][k & 1];
                const u32 t = pk_bf16(hd * bf_lo(ww), hd * bf_hi(ww));
                const u32 uu = pk_bf16(bf_lo(t) + bf_lo(bb), bf_hi(t) + bf_hi(bb));
                const float u0 = bf_lo(uu), u1 = bf_hi(uu);
                constexpr float C0 = -2.f * 0.7978845608028654f * 1.4426950408889634f, C1 = C0 * 0.044715f;
                const float z0 = u0 * fmaf(u0 * u0, C1, C0), z1 = u1 * fmaf(u1 * u1, C1, C0);
                const float g0 = u0 * __builtin_amdgcn_rcpf(1.f + __builtin_amdgcn_exp2f(z0));
                const float g1 = u1 * __builtin_amdgcn_rcpf(1.f + __builtin_amdgcn_exp2f(z1));
                y = dot2(pk_bf16(g0, g1), w2, y);
            }
            y = bfr(bfr(wave_sum(y)) + bf_lo(b2r));
            const float gd = bfr(y / (1.f + __expf(-y)));
            if ((lane & 15) == d) gate = gd;
        }
        {
            u32 ref = 0, have = 0, per = (u32)__builtin_amdgcn_s_memrealtime() >> PSHIFT;
            for (;;) {
                const u32 ld = __builtin_amdgcn_readfirstlane(*(volatile LAS u32*)(L + CT_LANDED));
                if (ld >= 2 * TPH - CSTOP || budget == 0) break;
                --budget;
                const u32 now = (u32)__builtin_amdgcn_s_memrealtime();
                const u32 lag = now * 10u - ld * TAU;
                if ((now >> PSHIFT) != per) {
                    per = now >> PSHIFT;
                    GAS char* pw = ws + WS_PROG + (per & 63) * (8 * PSTRIDE), *pr = ws + WS_PROG + ((per - 1) & 63) * (8 * PSTRIDE);
                    if (b == 0 && lane == 0) __hip_atomic_store((GAS u64*)(pw + (h & 7) * PSTRIDE + (h >> 3) * 8), ((u64)(tag0 + per) << 32) | lag, RLX_AGENT);
                    const u64 e = __hip_atomic_load((GAS u64*)(pr + (lane & 7) * PSTRIDE + ((lane >> 3) & 3) * 8), RLX_AGENT);
                    const bool ok = (u32)(e >> 32) == tag0 + per - 1;
                    const int n = __builtin_popcountll(__ballot(ok));
                    const float d = wave_sum(ok ? (float)(int)((u32)e - lag) : 0.f);
                    if (n >= 48) { ref = lag + __builtin_amdgcn_readfirstlane((int)(d / (float)n)); have = 1; }
                }
                if (have) {
                    const float dev = (float)(int)(lag - ref);
                    const float wf = fminf(fmaxf((float)WAITN + __builtin_rintf(dev * (1.f / CGAIN)), (float)WMIN), (float)WMAX);
                    *(volatile LAS u32*)(L + CT_WIN) = (u32)wf;
                }
                __builtin_amdgcn_s_sleep(32);
            }
        }
        asm volatile("s_barrier" ::: "memory");
        __builtin_amdgcn_s_sleep(28);
        CFENCE();
        __builtin_amdgcn_s_setprio(3);
        const u32 tag = __builtin_amdgcn_readfirstlane(*(volatile LAS u32*)(L + CT_TAG));
        GAS u64* in = (GAS u64*)(ws + WS_INBOX + (size_t)wg * 4096) + lane;
        u64 v[8];
#pragma unroll
        for (int k = 0; k < 8; ++k) v[k] = 0;
        u32 pend = 0xffu;
        for (;;) {
#pragma unroll
            for (int k = 0; k < 8; ++k)
                if (pend & (1u << k)) v[k] = __hip_atomic_load(in + k * 64, RLX_AGENT);
#pragma unroll
            for (int k = 0; k < 8; ++k)
                if (__all((u32)(v[k] >> 32) == tag)) pend &= ~(1u << k);
            if (pend == 0 || budget == 0) break;
            --budget;
        }
        float s = 0.f;
#pragma unroll
        for (int k = 0; k < 8; ++k) s += __uint_as_float((u32)v[k]);
        s = fold16(s, s);
        s = fold32(s, s);
        if (blockIdx.x == DGWG) {
            const u64 t0 = __builtin_amdgcn_s_memrealtime(), c1 = __builtin_amdgcn_s_memtime();
            const v4u st = *(volatile LAS v4u*)(L + CT_FLAG + 48);
            const float mhz = (float)(u32)(c1 - dg_c0) / (float)(u32)(t0 - dg_r0) * 100.f;
            const float kns = (float)(st.y - st.x) * (10.f / 448), vns = (float)(st.z - st.y) * (10.f / 512);
            const float t64 = st.x * 0.01f, tail = ((u32)(t0 - dg_r0) - st.z) * 0.01f, tot = (u32)(t0 - dg_r0) * 0.01f;
            const float us = DIAGUS;
            const u32 ticks = __builtin_amdgcn_readfirstlane((u32)fminf(fmaxf(us * 100.f, 0.f), 60000.f));
            while (__builtin_amdgcn_s_memrealtime() - t0 < (u64)ticks) __builtin_amdgcn_s_sleep(4);
        }
        const float res = bf_lo(xo) + bfr(bfr(s) * gate);
        if (lane < 16) out[b * DM + 16 * h + lane] = (u16)pk_bf16(res, res);
        if (h == 0 && lane == 0) __hip_atomic_store((GAS u32*)(ws + WS_EPOCH + b * 128), tag, RLX_AGENT);
    } else {
        __builtin_amdgcn_s_setprio(2);
        const int ci = wave - 1, r = lane >> 4, c = lane & 15;
        v4u xr = *(const GAS v4u*)(x + b * (DM * 2) + lane * 16);
        u32 tag = __hip_atomic_load((GAS u32*)(ws + WS_EPOCH + b * 128), RLX_AGENT);
        v4u w[32];
        const int rot = b;
        {
            const __amdgpu_buffer_rsrc_t rs = __builtin_amdgcn_make_buffer_rsrc((void*)wq_, 0, DM * NH * DH * 2, 0x00020000);
            const u32 vo = 32 * r * (NH * DH * 2) + c * 16, so = 128 * ci * (NH * DH * 2) + h * (DH * 2);
#pragma unroll
            for (int i = 0; i < 32; ++i)
                w[i] = __builtin_amdgcn_raw_buffer_load_b128(rs, vo, so + ((i + 4 * rot) & 31) * (NH * DH * 2), AUX_NT);
        }
        __builtin_amdgcn_sched_barrier(0);
        asm volatile("s_barrier" ::: "memory");
        asm volatile("" : "+v"(xr), "+v"(tag) :: "memory");
        tag += TAG_STEP;
        LAS char* priv = L + PV_OFF + ci * 1024;
        {
            float xv[8], xn[8];
            unpack8(xr, xv);
            ln8(xv, xn);
            v4u o;
#pragma unroll
            for (int e = 0; e < 4; ++e) o[e] = pk_bf16(xn[2 * e], xn[2 * e + 1]);
            v2u o0 = {o.x, o.y}, o1 = {o.z, o.w};
            *(LAS v2u*)(priv + (lane >> 2) * 64 + ((4 * (lane & 3) - 2 * rot) & 15) * 4) = o0;
            *(LAS v2u*)(priv + (lane >> 2) * 64 + ((4 * (lane & 3) + 2 - 2 * rot) & 15) * 4) = o1;
        }
        if (ci == 0) *(volatile LAS u32*)(L + CT_TAG) = tag;
        pair_rows(w);
        {
            const v2f qp = gemv32(w, (const LAS v4u*)(priv + (128 * ci + 32 * r) * 2));
            *(LAS v2f*)(L + QP_OFF + ci * 512 + (8 * c + 2 * r) * 4) = qp;
        }
        CFENCE();
        cbar(L, 0, ci, budget);
        u32 q[4];
        {
            v4f s0 = *(const LAS v4f*)(L + QP_OFF + c * 32), s1 = *(const LAS v4f*)(L + QP_OFF + c * 32 + 16);
#pragma unroll
            for (int k = 1; k < 4; ++k) { s0 += *(const LAS v4f*)(L + QP_OFF + k * 512 + c * 32); s1 += *(const LAS v4f*)(L + QP_OFF + k * 512 + c * 32 + 16); }
            q[0] = pk_bf16(s0.x, s0.y); q[1] = pk_bf16(s0.z, s0.w); q[2] = pk_bf16(s1.x, s1.y); q[3] = pk_bf16(s1.z, s1.w);
        }
        tag = *(volatile LAS u32*)(L + CT_TAG);
        volatile LAS u32* freew = (volatile LAS u32*)(L + CT_FREE + ci * 256 + lane * 4);
        LAS char* myscores = L + SC_OFF + ci * 4096;
        u32 landed = 0;
        float mx = -INFINITY;
        const bool lo8 = c < 8, lo4 = (c & 4) == 0;
        const __amdgpu_buffer_rsrc_t wors = __builtin_amdgcn_make_buffer_rsrc((void*)wo_, 0, DM * NH * DH * 2, 0x00020000);
        const u32 wovo = (h * DH + 32 * r) * (DM * 2) + (128 * ci + 8 * c) * 2;
#pragma unroll
        for (int seg = 0; seg < 8; ++seg) {
#pragma unroll
            for (int i = 4 * seg; i < 4 * seg + 4; ++i) {
                const u32 vo = wovo + (i & 3) * (DM * 2), so = ((seg + rot) & 7) * (4 * DM * 2);
                if (seg) w[i] = __builtin_amdgcn_raw_buffer_load_b128(wors, vo, so, 0);
                else w[i] = __builtin_amdgcn_raw_buffer_load_b128(wors, vo, so, AUX_NT);
            }
            if (seg == 0) {
                __builtin_amdgcn_sched_barrier(0);
#pragma unroll
                for (int i = 0; i < 4; ++i) w[28 + i] = __builtin_amdgcn_raw_buffer_load_b128(wors, wovo + i * (DM * 2), rot * (4 * DM * 2), AUX_SC0);
            }
            if (seg == 1) asm volatile("" :: "v"(w[28]), "v"(w[29]), "v"(w[30]), "v"(w[31]));
            __builtin_amdgcn_sched_barrier(0);
#pragma unroll 1
            for (int it = seg * (ITERS / 8); it < (seg + 1) * (ITERS / 8); ++it) {
                const u32 tile = 4 * it + ci;
                wait_landed(L, tile, landed, budget);
                const LAS v4u* tp = (const LAS v4u*)(L + RING_OFF + (tile & (NSLOT - 1)) * TILE + lane * 16);
                v4u k0 = tp[0], k1 = tp[64], k2 = tp[128], k3 = tp[192];
                LGKM0();
                *freew = it + 1;
                asm volatile("" : "+v"(k0), "+v"(k1), "+v"(k2), "+v"(k3) :: "memory");
                float a0 = 0.f, a1 = 0.f, a2 = 0.f, a3 = 0.f;
#pragma unroll
                for (int e = 0; e < 4; ++e) { a0 = dot2(k0[e], q[e], a0); a1 = dot2(k1[e], q[e], a1); a2 = dot2(k2[e], q[e], a2); a3 = dot2(k3[e], q[e], a3); }
                const float b0 = (lo8 ? a0 : a2) + dpp<XOR8>(lo8 ? a2 : a0);
                const float b1 = (lo8 ? a1 : a3) + dpp<XOR8>(lo8 ? a3 : a1);
                float s = (lo4 ? b0 : b1) + dpp<XOR7>(lo4 ? b1 : b0);
                s += dpp<XOR2>(s);
                s += dpp<XOR1>(s);
                const float sc = bfr(s) * 0.088388347648318440f;
                const u32 sb = pk_bf16(sc, sc);
                mx = fmaxf(mx, bf_lo(sb));
                *(LAS u16*)(myscores + it * 32 + r * 8 + (c >> 2) * 2) = (u16)sb;
            }
        }
        octet_rows(w);
        PIN16(w, 0); PIN16(w, 16);
        mx = wave_max(mx);
        *(volatile LAS float*)(L + CT_MAXV + ci * 4) = mx;
        cbar(L, 1, ci, budget);
        {
            const v4f mv = *(volatile LAS v4f*)(L + CT_MAXV);
            const float gm = fmaxf(fmaxf(mv.x, mv.y), fmaxf(mv.z, mv.w));
            LAS v4u* sp = (LAS v4u*)(myscores + lane * 16);
            float ex[32];
            float sum = 0.f;
#pragma unroll
            for (int j = 0; j < 4; ++j) {
                const v4u u = sp[j * 64];
#pragma unroll
                for (int e = 0; e < 4; ++e) {
                    const float e0 = __builtin_amdgcn_exp2f((bf_lo(u[e]) - gm) * 1.4426950408889634f);
                    const float e1 = __builtin_amdgcn_exp2f((bf_hi(u[e]) - gm) * 1.4426950408889634f);
                    ex[8 * j + 2 * e] = e0; ex[8 * j + 2 * e + 1] = e1;
                    sum += e0 + e1;
                }
            }
            sum = wave_sum(sum);
            *(volatile LAS float*)(L + CT_SUMV + ci * 4) = sum;
            cbar(L, 2, ci, budget);
            const v4f sv = *(volatile LAS v4f*)(L + CT_SUMV);
            const float inv = 1.f / ((sv.x + sv.y) + (sv.z + sv.w));
#pragma unroll
            for (int j = 0; j < 4; ++j) {
                v4u u;
#pragma unroll
                for (int e = 0; e < 4; ++e) u[e] = pk_bf16(ex[8 * j + 2 * e] * inv, ex[8 * j + 2 * e + 1] * inv);
                sp[j * 64] = u;
            }
        }
        CFENCE();
        float acc[8];
#pragma unroll
        for (int e = 0; e < 8; ++e) acc[e] = 0.f;
        u32 opw = OP_OFF + ci * 512 + (8 * c + 2 * r) * 4, opr = OP_OFF + (16 * r + ((c + 2 * rot) & 15)) * 8;
        GAS char* g = ws + WS_INBOX + ((size_t)((b * 32 + 8 * ci + (c >> 1)) * 32 + (h & 7) * 4 + (h >> 3))) * 128 + (8 * (c & 1) + 2 * r) * 8;
        asm volatile("" : "+v"(opw), "+v"(opr), "+v"(g));
#pragma unroll 1
        for (int it = 0; it < ITERS; ++it) {
            const u32 tile = TPH + 4 * it + ci;
            const v2u pp = *(const LAS v2u*)(myscores + it * 32 + r * 8);
            wait_landed(L, tile, landed, budget);
            const LAS v4u* tp = (const LAS v4u*)(L + RING_OFF + (tile & (NSLOT - 1)) * TILE + lane * 16);
            v4u v0 = tp[0], v1 = tp[64], v2 = tp[128], v3 = tp[192];
            LGKM0();
            *freew = ITERS + it + 1;
            asm volatile("" : "+v"(v0), "+v"(v1), "+v"(v2), "+v"(v3) :: "memory");
#pragma unroll
            for (int e = 0; e < 4; ++e) {
                acc[2 * e] = dot2(__builtin_amdgcn_perm(v1[e], v0[e], 0x05040100u), pp.x, acc[2 * e]);
                acc[2 * e + 1] = dot2(__builtin_amdgcn_perm(v1[e], v0[e], 0x07060302u), pp.x, acc[2 * e + 1]);
            }
#pragma unroll
            for (int e = 0; e < 4; ++e) {
                acc[2 * e] = dot2(__builtin_amdgcn_perm(v3[e], v2[e], 0x05040100u), pp.y, acc[2 * e]);
                acc[2 * e + 1] = dot2(__builtin_amdgcn_perm(v3[e], v2[e], 0x07060302u), pp.y, acc[2 * e + 1]);
            }
        }
        *(LAS v2f*)(size_t)opw = fold_rows(acc);
        asm volatile("s_waitcnt lgkmcnt(0)\n\ts_barrier" ::: "memory");
        const LAS char* op = (const LAS char*)(size_t)opr;
        const v2f o0 = *(const LAS v2f*)op, o1 = *(const LAS v2f*)(op + 512), o2 = *(const LAS v2f*)(op + 1024), o3 = *(const LAS v2f*)(op + 1536);
        const v2f o = (o0 + o1) + (o2 + o3);
        const v2f sp = gemv32_mfma(w, pk_bf16(o.x, o.y), r & 1, r & 2);
        store16_agent(g, (v4u){__float_as_uint(sp.x), tag, __float_as_uint(sp.y), tag});
    }
}

extern "C" void kernel_launch(void* const* d_in, const int* in_sizes, int n_in,
                              void* d_out, int out_size, void* d_ws, size_t ws_size,
                              hipStream_t stream) {
    (void)in_sizes; (void)n_in; (void)out_size; (void)ws_size;
    fused_decode_kernel<<<NB * NH, 384, 0, stream>>>(
        (const char*)d_in[1], (const char*)d_in[3], (const char*)d_in[0], (char*)d_ws, (const char*)d_in[2], (const char*)d_in[4],
        (const char*)d_in[6], (const char*)d_in[7], (const char*)d_in[8], (const char*)d_in[9], (u16*)d_out);
}
```

```cpp
#define DGWG 77
#define DGREF (fmn)
#include <hip/hip_runtime.h>
#include <hip/hip_bf16.h>
#include <stdint.h>

#define GAS __attribute__((address_space(1)))
#define LAS __attribute__((address_space(3)))
typedef unsigned u32;
typedef unsigned short u16;
typedef unsigned long long u64;
typedef u32 v4u __attribute__((ext_vector_type(4)));
typedef u32 v2u __attribute__((ext_vector_type(2)));
typedef float v4f __attribute__((ext_vector_type(4)));
typedef float v2f __attribute__((ext_vector_type(2)));
typedef __bf16 bf16x2 __attribute__((ext_vector_type(2)));
typedef __bf16 bf16x8 __attribute__((ext_vector_type(8)));

#define RLX_AGENT __ATOMIC_RELAXED, __HIP_MEMORY_SCOPE_AGENT

constexpr int NB = 8, NH = 32, SK = 8192, DH = 128, DM = 512;
constexpr int TILE = 4096, NSLOT = 32, TPH = SK * DH * 2 / TILE;
constexpr int ITERS = TPH / 4;
constexpr int RING_OFF = 0;
constexpr int SC_OFF = RING_OFF + NSLOT * TILE;
constexpr int QP_OFF = SC_OFF + SK * 2;
constexpr int OP_OFF = QP_OFF + 2048;
constexpr int PV_OFF = OP_OFF + 2048;
constexpr int GH_OFF = PV_OFF + 4096;
constexpr int CT_OFF = GH_OFF + 2048;
constexpr int CT_LANDED = CT_OFF;
constexpr int CT_FREE = CT_OFF + 256;
constexpr int CT_FLAG = CT_OFF + 1280;
constexpr int CT_MAXV = CT_FLAG + 64, CT_SUMV = CT_FLAG + 80, CT_TAG = CT_FLAG + 96, CT_WIN = CT_FLAG + 112;
constexpr int CT_BYTES = 1536;
constexpr int LDS_BYTES = CT_OFF + CT_BYTES;
static_assert(LDS_BYTES <= 160 * 1024 && LDS_BYTES > 80 * 1024, "one workgroup per CU");
constexpr size_t WS_INBOX = 0;
constexpr size_t WS_EPOCH = (size_t)NB * 32 * 32 * 128;
constexpr size_t WS_PROG = 4u << 20;
constexpr u32 TAG_STEP = 0x9E3779B9u;
constexpr u32 SPIN_CAP = 1u << 18;
constexpr int WAITN = 19, WMIN = WAITN - 2, WMAX = WAITN + 3;
constexpr int RUNLEN = 16;
constexpr int PRE = 5;
constexpr float CGAIN = 400.f;
constexpr int TAU = 134;
constexpr int PSHIFT = 9, PSTRIDE = 256, CSTOP = 24;
constexpr int AUX_SC0 = 1, AUX_NT = 2;

__device__ __forceinline__ float bf_lo(u32 u) { return __uint_as_float(u << 16); }
__device__ __forceinline__ float bf_hi(u32 u) { return __uint_as_float(u & 0xffff0000u); }
__device__ __forceinline__ u32 pk_bf16(float lo, float hi) {
    bf16x2 v = {(__bf16)lo, (__bf16)hi};
    return __builtin_bit_cast(u32, v);
}
__device__ __forceinline__ float bfr(float v) { return bf_lo(pk_bf16(v, v)); }
__device__ __forceinline__ float dot2(u32 a, u32 b, float acc) {
    return __builtin_amdgcn_fdot2_f32_bf16(__builtin_bit_cast(bf16x2, a), __builtin_bit_cast(bf16x2, b), acc, false);
}
template <int CTRL> __device__ __forceinline__ float dpp(float v) {
    return __builtin_bit_cast(float, __builtin_amdgcn_mov_dpp(__builtin_bit_cast(int, v), CTRL, 0xf, 0xf, true));
}
constexpr int XOR1 = 0xB1, XOR2 = 0x4E, XOR7 = 0x141, XOR8 = 0x128;
__device__ __forceinline__ float fold32(float a, float b) {
    auto s = __builtin_amdgcn_permlane32_swap(__float_as_uint(a), __float_as_uint(b), false, false);
    return __uint_as_float(s[0]) + __uint_as_float(s[1]);
}
__device__ __forceinline__ float fold16(float a, float b) {
    auto s = __builtin_amdgcn_permlane16_swap(__float_as_uint(a), __float_as_uint(b), false, false);
    return __uint_as_float(s[0]) + __uint_as_float(s[1]);
}
__device__ __forceinline__ float wave_sum(float v) {
    v += dpp<XOR1>(v); v += dpp<XOR2>(v); v += dpp<XOR7>(v); v += dpp<XOR8>(v);
    v = fold16(v, v); return fold32(v, v);
}
__device__ __forceinline__ float wave_max(float v) {
    v = fmaxf(v, dpp<XOR1>(v)); v = fmaxf(v, dpp<XOR2>(v)); v = fmaxf(v, dpp<XOR7>(v)); v = fmaxf(v, dpp<XOR8>(v));
    auto s = __builtin_amdgcn_permlane16_swap(__float_as_uint(v), __float_as_uint(v), false, false);
    v = fmaxf(__uint_as_float(s[0]), __uint_as_float(s[1]));
    auto t = __builtin_amdgcn_permlane32_swap(__float_as_uint(v), __float_as_uint(v), false, false);
    return fmaxf(__uint_as_float(t[0]), __uint_as_float(t[1]));
}
__device__ __forceinline__ v2f fold_rows(const float (&a)[8]) {
    const float x0 = fold32(a[0], a[4]), x1 = fold32(a[1], a[5]), x2 = fold32(a[2], a[6]), x3 = fold32(a[3], a[7]);
    v2f o; o.x = fold16(x0, x2); o.y = fold16(x1, x3); return o;
}
__device__ __forceinline__ void ln8(const float (&v)[8], float (&o)[8]) {
    float s = 0.f;
#pragma unroll
    for (int i = 0; i < 8; ++i) s += v[i];
    const float mean = wave_sum(s) * (1.f / DM);
    float q = 0.f;
#pragma unroll
    for (int i = 0; i < 8; ++i) { const float d = v[i] - mean; q = fmaf(d, d, q); }
    const float var = bfr(wave_sum(q) * (1.f / DM));
    const float m = bfr(mean);
    const float den = bfr(__builtin_amdgcn_sqrtf(bfr(var + 1e-5f)));
    const float rc = __builtin_amdgcn_rcpf(den);
#pragma unroll
    for (int i = 0; i < 8; ++i) {
        const float d = bfr(v[i] - m), t = d * rc;
        o[i] = fmaf(fmaf(-t, den, d), rc, t);
    }
}
__device__ __forceinline__ void unpack8(v4u u, float (&v)[8]) {
#pragma unroll
    for (int e = 0; e < 4; ++e) { v[2 * e] = bf_lo(u[e]); v[2 * e + 1] = bf_hi(u[e]); }
}
__device__ __forceinline__ void store16_agent(GAS char* p, v4u v) {
    asm volatile("global_store_dwordx4 %0, %1, off sc1\n\ts_nop 1" :: "v"(p), "v"(v) : "memory");
}
#define LGKM0() asm volatile("s_waitcnt lgkmcnt(0)" ::: "memory")
#define CFENCE() asm volatile("" ::: "memory")
#define PIN16(w, o) asm volatile("" : "+v"(w[o + 0]), "+v"(w[o + 1]), "+v"(w[o + 2]), "+v"(w[o + 3]), "+v"(w[o + 4]), "+v"(w[o + 5]), "+v"(w[o + 6]), "+v"(w[o + 7]), \
                                      "+v"(w[o + 8]), "+v"(w[o + 9]), "+v"(w[o + 10]), "+v"(w[o + 11]), "+v"(w[o + 12]), "+v"(w[o + 13]), "+v"(w[o + 14]), "+v"(w[o + 15]) :: "memory")

__device__ __forceinline__ void pair_rows(v4u (&w)[32]) {
#pragma unroll
    for (int i = 0; i < 16; ++i)
#pragma unroll
        for (int e = 0; e < 4; ++e) {
            const u32 a = w[2 * i][e], b = w[2 * i + 1][e];
            w[2 * i][e] = __builtin_amdgcn_perm(b, a, 0x05040100u);
            w[2 * i + 1][e] = __builtin_amdgcn_perm(b, a, 0x07060302u);
        }
}
__device__ __forceinline__ v2f gemv32(const v4u (&w)[32], const LAS v4u* vec  ) {
    v4u p[4];
#pragma unroll
    for (int j = 0; j < 4; ++j) p[j] = vec[j];
    float a[8];
#pragma unroll
    for (int e = 0; e < 8; ++e) a[e] = 0.f;
#pragma unroll
    for (int i = 0; i < 16; ++i)
#pragma unroll
        for (int e = 0; e < 4; ++e) {
            a[2 * e] = dot2(w[2 * i][e], p[i >> 2][i & 3], a[2 * e]);
            a[2 * e + 1] = dot2(w[2 * i + 1][e], p[i >> 2][i & 3], a[2 * e + 1]);
        }
    return fold_rows(a);
}
__device__ __forceinline__ void octet_rows(v4u (&w)[32]) {
    v4u o[32];
#pragma unroll
    for (int j = 0; j < 4; ++j)
#pragma unroll
        for (int t = 0; t < 4; ++t)
#pragma unroll
            for (int e = 0; e < 4; ++e) {
                const u32 a = w[8 * j + 2 * t][e], b = w[8 * j + 2 * t + 1][e];
                o[4 * (2 * e) + j][t] = __builtin_amdgcn_perm(b, a, 0x05040100u);
                o[4 * (2 * e + 1) + j][t] = __builtin_amdgcn_perm(b, a, 0x07060302u);
            }
#pragma unroll
    for (int i = 0; i < 32; ++i) w[i] = o[i];
}
template <int J> __device__ __forceinline__ v4u bcast4(u32 pr) {
    return (v4u){(u32)__builtin_amdgcn_mov_dpp(pr, 0x150 + 4 * J, 0xf, 0xf, true), (u32)__builtin_amdgcn_mov_dpp(pr, 0x151 + 4 * J, 0xf, 0xf, true),
                 (u32)__builtin_amdgcn_mov_dpp(pr, 0x152 + 4 * J, 0xf, 0xf, true), (u32)__builtin_amdgcn_mov_dpp(pr, 0x153 + 4 * J, 0xf, 0xf, true)};
}
__device__ __forceinline__ v2f gemv32_mfma(const v4u (&w)[32], u32 pr, bool r1, bool r2) {
    const v4u p[4] = {bcast4<0>(pr), bcast4<1>(pr), bcast4<2>(pr), bcast4<3>(pr)};
    v4f a[8];
#pragma unroll
    for (int e = 0; e < 8; ++e) a[e] = (v4f){0.f, 0.f, 0.f, 0.f};
#pragma unroll
    for (int j = 0; j < 4; ++j)
#pragma unroll
        for (int e = 0; e < 8; ++e)
            a[e] = __builtin_amdgcn_mfma_f32_16x16x32_bf16(__builtin_bit_cast(bf16x8, p[j]), __builtin_bit_cast(bf16x8, w[4 * e + j]), a[e], 0, 0, 0);
    v2f o;
    o.x = r2 ? (r1 ? a[6].x : a[4].x) : (r1 ? a[2].x : a[0].x);
    o.y = r2 ? (r1 ? a[7].x : a[5].x) : (r1 ? a[3].x : a[1].x);
    return o;
}
__device__ __forceinline__ void cbar(LAS char* L, int which, int ci, u32& budget) {
    LGKM0();
    *(volatile LAS u32*)(L + CT_FLAG + which * 16 + ci * 4) = 1u;
    for (;;) {
        const v4u f = *(volatile LAS v4u*)(L + CT_FLAG + which * 16);
        if (__builtin_amdgcn_readfirstlane(f.x & f.y & f.z & f.w)) break;
        if (budget == 0) break;
        --budget;
    }
    CFENCE();
}
__device__ __forceinline__ void wait_landed(LAS char* L, u32 tile, u32& landed, u32& budget) {
    while (landed <= tile) {
        landed = __builtin_amdgcn_readfirstlane(*(volatile LAS u32*)(L + CT_LANDED));
        if (landed > tile || budget == 0) break;
        --budget;
        __builtin_amdgcn_s_sleep(1);
    }
    CFENCE();
}

constexpr int ABIT[13] = {8, 10, 13, 16, 9, 11, 19, 15, 18, 17, 20, 12, 14};
constexpr u32 abits(u32 v, int first, int n) {
    u32 o = 0;
    for (int i = 0; i < n; ++i) o |= (v >> i & 1) << ABIT[first + i];
    return o;
}
static_assert(abits(0x1fff, 0, 13) == 0x1fff00, "a permutation of the row bits");
template <int FIRST, int N> __device__ __forceinline__ u32 abits_of(u32 v) {
    u32 o = 0;
#pragma unroll
    for (int i = 0; i < N; ++i) o |= (v >> i & 1) << ABIT[FIRST + i];
    return o;
}
constexpr u64 PRE_OFFS = (u64)(abits(1, 4, 9) >> 8) | (u64)(abits(2, 4, 9) >> 8) << 16 | (u64)(abits(3, 4, 9) >> 8) << 32 | (u64)(abits(4, 4, 9) >> 8) << 48;
static_assert(PRE <= 5 && RUNLEN == 16, "prologue offsets, run table");
constexpr int LAGE = (WMAX + 3) / 4;
static_assert(RING_OFF == 0 && NSLOT * TILE == 0x20000 && WMAX + 4 <= 63 && WMAX == WMIN + 5 && 4 * PRE <= 63, "loader loop constants");
__device__ __forceinline__ void stream_tiles(u32 win, const GAS char* src, u32 tab, u32 li, const u32 (&vo)[4], u32 t, u32 end, u32 pub_addr) {
    u32 tmp, n = end - t, pub = t - ((win + 3) >> 2);
    const u32 slot = RING_OFF + (t & (NSLOT - 1)) * TILE;
    const u32 skip = 4 * (win - WMIN);
    const u32 slo = (u32)(size_t)src, shi = (u32)((size_t)src >> 32);
    asm volatile("v_readlane_b32 s44, %[tab], %[li]\n\t"
                 "s_getpc_b64 s[40:41]\n"
                 "1:\n\t"
                 "s_add_u32 s40, s40, %[skip]\n\t"
                 "s_addc_u32 s41, s41, 0\n\t"
                 "s_add_u32 s40, s40, 2f-1b\n\t"
                 "s_addc_u32 s41, s41, 0\n\t"
                 "s_mov_b32 m0, %[slot]\n\t"
                 "s_add_u32 s42, %[slo], s44\n\t"
                 "s_addc_u32 s43, %[shi], 0\n\t"
                 "s_setpc_b64 s[40:41]\n"
                 "2:\n\t"
                 "s_waitcnt vmcnt(%c[w0])\n\t"
                 "s_waitcnt vmcnt(%c[w1])\n\t"
                 "s_waitcnt vmcnt(%c[w2])\n\t"
                 "s_waitcnt vmcnt(%c[w3])\n\t"
                 "s_waitcnt vmcnt(%c[w4])\n\t"
                 "s_waitcnt vmcnt(%c[w5])\n\t"
                 "global_load_lds_dwordx4 %[v0], s[42:43] nt\n\t"
                 "global_load_lds_dwordx4 %[v1], s[42:43] offset:1024 nt\n\t"
                 "global_load_lds_dwordx4 %[v2], s[42:43] offset:2048 nt\n\t"
                 "global_load_lds_dwordx4 %[v3], s[42:43] offset:3072 nt\n\t"
                 "v_max_i32 %[tmp], 0, %[pub]\n\t"
                 "s_add_u32 %[li], %[li], 1\n\t"
                 "ds_write_b32 %[pa], %[tmp]\n\t"
                 "v_add_u32 %[pub], 1, %[pub]\n\t"
                 "v_readlane_b32 s44, %[tab], %[li]\n\t"
                 "s_add_u32 m0, m0, 0x1000\n\t"
                 "s_and_b32 m0, m0, 0x1f000\n\t"
                 "s_add_u32 s42, %[slo], s44\n\t"
                 "s_addc_u32 s43, %[shi], 0\n\t"
                 "s_sub_u32 %[n], %[n], 1\n\t"
                 "s_cmp_eq_u32 %[n], 0\n\t"
                 "s_cbranch_scc1 3f\n\t"
                 "s_setpc_b64 s[40:41]\n"
                 "3:"
                 : [pub] "+v"(pub), [n] "+s"(n), [li] "+s"(li), [tmp] "=&v"(tmp)
                 : [slo] "s"(slo), [shi] "s"(shi), [pa] "v"(pub_addr), [slot] "s"(slot), [skip] "s"(skip), [tab] "v"(tab),
                   [v0] "v"(vo[0]), [v1] "v"(vo[1]), [v2] "v"(vo[2]), [v3] "v"(vo[3]),
                   [w0] "i"(WMIN), [w1] "i"(WMIN + 1), [w2] "i"(WMIN + 2), [w3] "i"(WMIN + 3), [w4] "i"(WMIN + 4), [w5] "i"(WMIN + 5)
                 : "scc", "s40", "s41", "s42", "s43", "s44", "memory");
}
template <int N> __device__ __forceinline__ void publish_tail(u32 pub_addr) {
    asm volatile("s_waitcnt vmcnt(%c2)\n\tds_write_b32 %0, %1" :: "v"(pub_addr), "v"(2 * TPH - N), "i"(4 * N) : "memory");
    if constexpr (N > 0) publish_tail<N - 1>(pub_addr);
}

__global__ void __launch_bounds__(384, 2)
fused_decode_kernel(const char* __restrict__ kc_, const char* __restrict__ wq_, const char* __restrict__ x_, char* ws_,
                    const char* __restrict__ vc_, const char* __restrict__ wo_,
                    const char* __restrict__ w1_, const char* __restrict__ b1_, const char* __restrict__ w2_,
                    const char* __restrict__ b2_, u16* __restrict__ out_) {
    u64 dg_r0;
    asm volatile("s_memrealtime %0\n\ts_waitcnt lgkmcnt(0)" : "=s"(dg_r0) :: "memory"); asm volatile("" :: "s"(kc_), "s"(wq_), "s"(x_), "s"(ws_) : "memory");
    const GAS char* x = (const GAS char*)x_; const GAS char* kc = (const GAS char*)kc_; const GAS char* vc = (const GAS char*)vc_;
    const GAS char* w1 = (const GAS char*)w1_; const GAS char* b1 = (const GAS char*)b1_; const GAS char* w2 = (const GAS char*)w2_;
    const GAS char* b2 = (const GAS char*)b2_; GAS u16* out = (GAS u16*)out_; GAS char* ws = (GAS char*)ws_;
    __shared__ __attribute__((aligned(1024))) char smem[LDS_BYTES];
    LAS char* L = (LAS char*)smem;
    const int lane = threadIdx.x & 63;
    const int wave = __builtin_amdgcn_readfirstlane(threadIdx.x >> 6);
    const int h = blockIdx.x & 31, b = blockIdx.x >> 5;
    const int wg = b * 32 + h;
    u32 budget = SPIN_CAP;

    const GAS char* src = kc + (size_t)wg * (SK * DH * 2) - 3072;
    u32 vo[4];
#pragma unroll
    for (int g = 0; g < 4; ++g)
        vo[g] = (lane & 15) * 16 + abits_of<0, 2>(lane >> 4) + abits(g, 2, 2) + (3 - g) * 1024;
    {
        u64 pk = PRE_OFFS; u32 xo = 0;
        asm volatile("s_cmp_lg_u32 %[wave], 0\n\t"
                     "s_cbranch_scc1 2f\n\t"
                     "s_mov_b32 m0, 0\n\t"
                     "s_setprio 3\n"
                     "1:\n\t"
                     "s_add_u32 s42, %[slo], %[x]\n\t"
                     "s_addc_u32 s43, %[shi], 0\n\t"
                     "s_and_b32 %[x], s44, 0xffff\n\t"
                     "global_load_lds_dwordx4 %[v0], s[42:43] nt\n\t"
                     "global_load_lds_dwordx4 %[v1], s[42:43] offset:1024 nt\n\t"
                     "global_load_lds_dwordx4 %[v2], s[42:43] offset:2048 nt\n\t"
                     "global_load_lds_dwordx4 %[v3], s[42:43] offset:3072 nt\n\t"
                     "s_add_u32 m0, m0, 0x1000\n\t"
                     "s_lshl_b32 %[x], %[x], 8\n\t"
                     "s_lshr_b64 s[44:45], s[44:45], 16\n\t"
                     "s_cmp_lg_u32 m0, %c[end]\n\t"
                     "s_cbranch_scc1 1b\n"
                     "2:"
                     : [x] "+s"(xo), "+{s[44:45]}"(pk) : [slo] "s"((u32)(size_t)src), [shi] "s"((u32)((size_t)src >> 32)), [wave] "s"(wave), [end] "i"(PRE * TILE),
                       [v0] "v"(vo[0]), [v1] "v"(vo[1]), [v2] "v"(vo[2]), [v3] "v"(vo[3]), "s"(wq_), "s"(x_), "s"(ws_) : "scc", "s42", "s43", "memory");
    }

    if (wave == 0) {
        const u32 lbase = (u32)(size_t)L;
        asm volatile("s_barrier" ::: "memory");
        const u32 pub_addr = lbase + CT_LANDED + lane * 4;
        const u32 free_addr = lbase + CT_FREE;
        const u32 tlo = abits_of<4, 4>(lane), thi = abits_of<8, 5>(lane);
        u32 t = PRE;
        for (;;) {
            v2u f0, f1; u32 wv;
            asm volatile("ds_read2st64_b32 %0, %3 offset0:0 offset1:1\n\tds_read2st64_b32 %1, %3 offset0:2 offset1:3\n\t"
                         "ds_read_b32 %2, %3 offset:%c4\n\ts_waitcnt lgkmcnt(0)"
                         : "=&v"(f0), "=&v"(f1), "=&v"(wv) : "v"(free_addr), "i"(CT_WIN - CT_FREE) : "memory");
            u32 limit = 4 * __builtin_amdgcn_readfirstlane(min(min(f0.x, f0.y), min(f1.x, f1.y))) + NSLOT;
            const u32 win = __builtin_amdgcn_readfirstlane(min(max(wv, (u32)WMIN), (u32)WMAX));
            if (t >= limit) {
                if (budget != 0) { --budget; __builtin_amdgcn_s_sleep(1); continue; }
                limit = ~0u;
            }
            const u32 end = min(limit, (t | (RUNLEN - 1)) + 1);
            stream_tiles(win, src + (u32)__builtin_amdgcn_readlane(thi, (t >> 4) & 31), tlo, t & 15, vo, t, end, pub_addr);
            t = end;
            if (t == TPH) src = vc + (size_t)wg * (SK * DH * 2) - 3072;
            if (t == 2 * TPH) break;
        }
        publish_tail<LAGE - 1>(pub_addr);
        asm volatile("s_waitcnt lgkmcnt(0)\n\ts_barrier" ::: "memory");
    } else if (wave == 5) {
        {
            const v4u z = {0u, 0u, 0u, 0u};
            volatile LAS v4u* cw = (volatile LAS v4u*)(L + CT_OFF);
            cw[lane] = z;
            if (lane < CT_BYTES / 16 - 64) cw[64 + lane] = z;
            *(volatile LAS u32*)(L + CT_WIN) = WAITN;
        }
        LGKM0();
        asm volatile("s_barrier" ::: "memory");
        __builtin_amdgcn_sched_barrier(0);
        const v4u xr = *(const GAS v4u*)(x + b * (DM * 2) + lane * 16);
        const u32 xo = *(const GAS u16*)(x + (b * DM + 16 * h + (lane & 15)) * 2);
        const u32 b2r = *(const GAS u16*)b2;
        asm volatile("s_waitcnt lgkmcnt(0)" : "+s"(dg_r0));
        if (lane == 0) __hip_atomic_store((GAS u32*)(ws + (5u << 20)) + blockIdx.x, (u32)dg_r0, RLX_AGENT);
        const u32 tag0 = __hip_atomic_load((GAS u32*)(ws + WS_EPOCH), RLX_AGENT) + TAG_STEP;
        v2u w1r[7], b1r[7], w2r[7];
#pragma unroll
        for (int k = 0; k < 7; ++k) {
            w1r[k] = *(const GAS v2u*)(w1 + lane * 56 + k * 8);
            b1r[k] = *(const GAS v2u*)(b1 + lane * 56 + k * 8);
            w2r[k] = *(const GAS v2u*)(w2 + lane * 56 + k * 8);
        }
        {
            float xv[8], xn[8], hv[8];
            unpack8(xr, xv);
            ln8(xv, xn);
#pragma unroll
            for (int i = 0; i < 8; ++i) xn[i] = bfr(xn[i]);
            ln8(xn, hv);
#pragma unroll
            for (int i = 0; i < 8; ++i) hv[i] = bfr(hv[i]);
            v4f a = {hv[0], hv[1], hv[2], hv[3]}, bb = {hv[4], hv[5], hv[6], hv[7]};
            *(LAS v4f*)(L + GH_OFF + lane * 32) = a;
            *(LAS v4f*)(L + GH_OFF + lane * 32 + 16) = bb;
        }
        float gate = 0.f;
#pragma unroll 1
        for (int d = 0; d < 16; ++d) {
            const float hd = *(const LAS float*)(L + GH_OFF + (16 * h + d) * 4);
            float y = 0.f;
#pragma unroll
            for (int k = 0; k < 14; ++k) {
                const u32 ww = w1r[k >> 1][k & 1], bb = b1r[k >> 1][k & 1], w2 = w2r[k >> 1][k & 1];
                const u32 t = pk_bf16(hd * bf_lo(ww), hd * bf_hi(ww));
                const u32 uu = pk_bf16(bf_lo(t) + bf_lo(bb), bf_hi(t) + bf_hi(bb));
                const float u0 = bf_lo(uu), u1 = bf_hi(uu);
                constexpr float C0 = -2.f * 0.7978845608028654f * 1.4426950408889634f, C1 = C0 * 0.044715f;
                const float z0 = u0 * fmaf(u0 * u0, C1, C0), z1 = u1 * fmaf(u1 * u1, C1, C0);
                const float g0 = u0 * __builtin_amdgcn_rcpf(1.f + __builtin_amdgcn_exp2f(z0));
                const float g1 = u1 * __builtin_amdgcn_rcpf(1.f + __builtin_amdgcn_exp2f(z1));
                y = dot2(pk_bf16(g0, g1), w2, y);
            }
            y = bfr(bfr(wave_sum(y)) + bf_lo(b2r));
            const float gd = bfr(y / (1.f + __expf(-y)));
            if ((lane & 15) == d) gate = gd;
        }
        {
            u32 ref = 0, have = 0, per = (u32)__builtin_amdgcn_s_memrealtime() >> PSHIFT;
            for (;;) {
                const u32 ld = __builtin_amdgcn_readfirstlane(*(volatile LAS u32*)(L + CT_LANDED));
                if (ld >= 2 * TPH - CSTOP || budget == 0) break;
                --budget;
                const u32 now = (u32)__builtin_amdgcn_s_memrealtime();
                const u32 lag = now * 10u - ld * TAU;
                if ((now >> PSHIFT) != per) {
                    per = now >> PSHIFT;
                    GAS char* pw = ws + WS_PROG + (per & 63) * (8 * PSTRIDE), *pr = ws + WS_PROG + ((per - 1) & 63) * (8 * PSTRIDE);
                    if (b == 0 && lane == 0) __hip_atomic_store((GAS u64*)(pw + (h & 7) * PSTRIDE + (h >> 3) * 8), ((u64)(tag0 + per) << 32) | lag, RLX_AGENT);
                    const u64 e = __hip_atomic_load((GAS u64*)(pr + (lane & 7) * PSTRIDE + ((lane >> 3) & 3) * 8), RLX_AGENT);
                    const bool ok = (u32)(e >> 32) == tag0 + per - 1;
                    const int n = __builtin_popcountll(__ballot(ok));
                    const float d = wave_sum(ok ? (float)(int)((u32)e - lag) : 0.f);
                    if (n >= 48) { ref = lag + __builtin_amdgcn_readfirstlane((int)(d / (float)n)); have = 1; }
                }
                if (have) {
                    const float dev = (float)(int)(lag - ref);
                    const float wf = fminf(fmaxf((float)WAITN + __builtin_rintf(dev * (1.f / CGAIN)), (float)WMIN), (float)WMAX);
                    *(volatile LAS u32*)(L + CT_WIN) = (u32)wf;
                }
                __builtin_amdgcn_s_sleep(32);
            }
        }
        asm volatile("s_barrier" ::: "memory");
        __builtin_amdgcn_s_sleep(28);
        CFENCE();
        __builtin_amdgcn_s_setprio(3);
        const u32 tag = __builtin_amdgcn_readfirstlane(*(volatile LAS u32*)(L + CT_TAG));
        GAS u64* in = (GAS u64*)(ws + WS_INBOX + (size_t)wg * 4096) + lane;
        u64 v[8];
#pragma unroll
        for (int k = 0; k < 8; ++k) v[k] = 0;
        u32 pend = 0xffu;
        for (;;) {
#pragma unroll
            for (int k = 0; k < 8; ++k)
                if (pend & (1u << k)) v[k] = __hip_atomic_load(in + k * 64, RLX_AGENT);
#pragma unroll
            for (int k = 0; k < 8; ++k)
                if (__all((u32)(v[k] >> 32) == tag)) pend &= ~(1u << k);
            if (pend == 0 || budget == 0) break;
            --budget;
        }
        float s = 0.f;
#pragma unroll
        for (int k = 0; k < 8; ++k) s += __uint_as_float((u32)v[k]);
        s = fold16(s, s);
        s = fold32(s, s);
        if (blockIdx.x == DGWG) {
            int mn = 0, mxv = 0;
            for (int k = 0; k < 4; ++k) { const int d = (int)(__hip_atomic_load((GAS u32*)(ws + (5u << 20)) + k * 64 + lane, RLX_AGENT) - (u32)dg_r0); mn = min(mn, d); mxv = max(mxv, d); }
            const float fmn = -wave_max(-(float)mn), fmx = wave_max((float)mxv);
            const int ref = __builtin_amdgcn_readfirstlane((int)(DGREF));
            while ((int)((u32)__builtin_amdgcn_s_memrealtime() - (u32)dg_r0) < 20000 + ref) __builtin_amdgcn_s_sleep(1);
        }
        const float res = bf_lo(xo) + bfr(bfr(s) * gate);
        if (lane < 16) out[b * DM + 16 * h + lane] = (u16)pk_bf16(res, res);
        if (h == 0 && lane == 0) __hip_atomic_store((GAS u32*)(ws + WS_EPOCH + b * 128), tag, RLX_AGENT);
    } else {
        __builtin_amdgcn_s_setprio(2);
        const int ci = wave - 1, r = lane >> 4, c = lane & 15;
        v4u xr = *(const GAS v4u*)(x + b * (DM * 2) + lane * 16);
        u32 tag = __hip_atomic_load((GAS u32*)(ws + WS_EPOCH + b * 128), RLX_AGENT);
        v4u w[32];
        const int rot = b;
        {
            const __amdgpu_buffer_rsrc_t rs = __builtin_amdgcn_make_buffer_rsrc((void*)wq_, 0, DM * NH * DH * 2, 0x00020000);
            const u32 vo = 32 * r * (NH * DH * 2) + c * 16, so = 128 * ci * (NH * DH * 2) + h * (DH * 2);
#pragma unroll
            for (int i = 0; i < 32; ++i)
                w[i] = __builtin_amdgcn_raw_buffer_load_b128(rs, vo, so + ((i + 4 * rot) & 31) * (NH * DH * 2), AUX_NT);
        }
        __builtin_amdgcn_sched_barrier(0);
        asm volatile("s_barrier" ::: "memory");
        asm volatile("" : "+v"(xr), "+v"(tag) :: "memory");
        tag += TAG_STEP;
        LAS char* priv = L + PV_OFF + ci * 1024;
        {
            float xv[8], xn[8];
            unpack8(xr, xv);
            ln8(xv, xn);
            v4u o;
#pragma unroll
            for (int e = 0; e < 4; ++e) o[e] = pk_bf16(xn[2 * e], xn[2 * e + 1]);
            v2u o0 = {o.x, o.y}, o1 = {o.z, o.w};
            *(LAS v2u*)(priv + (lane >> 2) * 64 + ((4 * (lane & 3) - 2 * rot) & 15) * 4) = o0;
            *(LAS v2u*)(priv + (lane >> 2) * 64 + ((4 * (lane & 3) + 2 - 2 * rot) & 15) * 4) = o1;
        }
        if (ci == 0) *(volatile LAS u32*)(L + CT_TAG) = tag;
        pair_rows(w);
        {
            const v2f qp = gemv32(w, (const LAS v4u*)(priv + (128 * ci + 32 * r) * 2));
            *(LAS v2f*)(L + QP_OFF + ci * 512 + (8 * c + 2 * r) * 4) = qp;
        }
        CFENCE();
        cbar(L, 0, ci, budget);
        u32 q[4];
        {
            v4f s0 = *(const LAS v4f*)(L + QP_OFF + c * 32), s1 = *(const LAS v4f*)(L + QP_OFF + c * 32 + 16);
#pragma unroll
            for (int k = 1; k < 4; ++k) { s0 += *(const LAS v4f*)(L + QP_OFF + k * 512 + c * 32); s1 += *(const LAS v4f*)(L + QP_OFF + k * 512 + c * 32 + 16); }
            q[0] = pk_bf16(s0.x, s0.y); q[1] = pk_bf16(s0.z, s0.w); q[2] = pk_bf16(s1.x, s1.y); q[3] = pk_bf16(s1.z, s1.w);
        }
        tag = *(volatile LAS u32*)(L + CT_TAG);
        volatile LAS u32* freew = (volatile LAS u32*)(L + CT_FREE + ci * 256 + lane * 4);
        LAS char* myscores = L + SC_OFF + ci * 4096;
        u32 landed = 0;
        float mx = -INFINITY;
        const bool lo8 = c < 8, lo4 = (c & 4) == 0;
        const __amdgpu_buffer_rsrc_t wors = __builtin_amdgcn_make_buffer_rsrc((void*)wo_, 0, DM * NH * DH * 2, 0x00020000);
        const u32 wovo = (h * DH + 32 * r) * (DM * 2) + (128 * ci + 8 * c) * 2;
#pragma unroll
        for (int seg = 0; seg < 8; ++seg) {
#pragma unroll
            for (int i = 4 * seg; i < 4 * seg + 4; ++i) {
                const u32 vo = wovo + (i & 3) * (DM * 2), so = ((seg + rot) & 7) * (4 * DM * 2);
                if (seg) w[i] = __builtin_amdgcn_raw_buffer_load_b128(wors, vo, so, 0);
                else w[i] = __builtin_amdgcn_raw_buffer_load_b128(wors, vo, so, AUX_NT);
            }
            if (seg == 0) {
                __builtin_amdgcn_sched_barrier(0);
#pragma unroll
                for (int i = 0; i < 4; ++i) w[28 + i] = __builtin_amdgcn_raw_buffer_load_b128(wors, wovo + i * (DM * 2), rot * (4 * DM * 2), AUX_SC0);
            }
            if (seg == 1) asm volatile("" :: "v"(w[28]), "v"(w[29]), "v"(w[30]), "v"(w[31]));
            __builtin_amdgcn_sched_barrier(0);
#pragma unroll 1
            for (int it = seg * (ITERS / 8); it < (seg + 1) * (ITERS / 8); ++it) {
                const u32 tile = 4 * it + ci;
                wait_landed(L, tile, landed, budget);
                const LAS v4u* tp = (const LAS v4u*)(L + RING_OFF + (tile & (NSLOT - 1)) * TILE + lane * 16);
                v4u k0 = tp[0], k1 = tp[64], k2 = tp[128], k3 = tp[192];
                LGKM0();
                *freew = it + 1;
                asm volatile("" : "+v"(k0), "+v"(k1), "+v"(k2), "+v"(k3) :: "memory");
                float a0 = 0.f, a1 = 0.f, a2 = 0.f, a3 = 0.f;
#pragma unroll
                for (int e = 0; e < 4; ++e) { a0 = dot2(k0[e], q[e], a0); a1 = dot2(k1[e], q[e], a1); a2 = dot2(k2[e], q[e], a2); a3 = dot2(k3[e], q[e], a3); }
                const float b0 = (lo8 ? a0 : a2) + dpp<XOR8>(lo8 ? a2 : a0);
                const float b1 = (lo8 ? a1 : a3) + dpp<XOR8>(lo8 ? a3 : a1);
                float s = (lo4 ? b0 : b1) + dpp<XOR7>(lo4 ? b1 : b0);
                s += dpp<XOR2>(s);
                s += dpp<XOR1>(s);
                const float sc = bfr(s) * 0.088388347648318440f;
                const u32 sb = pk_bf16(sc, sc);
                mx = fmaxf(mx, bf_lo(sb));
                *(LAS u16*)(myscores + it * 32 + r * 8 + (c >> 2) * 2) = (u16)sb;
            }
        }
        octet_rows(w);
        PIN16(w, 0); PIN16(w, 16);
        mx = wave_max(mx);
        *(volatile LAS float*)(L + CT_MAXV + ci * 4) = mx;
        cbar(L, 1, ci, budget);
        {
            const v4f mv = *(volatile LAS v4f*)(L + CT_MAXV);
            const float gm = fmaxf(fmaxf(mv.x, mv.y), fmaxf(mv.z, mv.w));
            LAS v4u* sp = (LAS v4u*)(myscores + lane * 16);
            float ex[32];
            float sum = 0.f;
#pragma unroll
            for (int j = 0; j < 4; ++j) {
                const v4u u = sp[j * 64];
#pragma unroll
                for (int e = 0; e < 4; ++e) {
                    const float e0 = __builtin_amdgcn_exp2f((bf_lo(u[e]) - gm) * 1.4426950408889634f);
                    const float e1 = __builtin_amdgcn_exp2f((bf_hi(u[e]) - gm) * 1.4426950408889634f);
                    ex[8 * j + 2 * e] = e0; ex[8 * j + 2 * e + 1] = e1;
                    sum += e0 + e1;
                }
            }
            sum = wave_sum(sum);
            *(volatile LAS float*)(L + CT_SUMV + ci * 4) = sum;
            cbar(L, 2, ci, budget);
            const v4f sv = *(volatile LAS v4f*)(L + CT_SUMV);
            const float inv = 1.f / ((sv.x + sv.y) + (sv.z + sv.w));
#pragma unroll
            for (int j = 0; j < 4; ++j) {
                v4u u;
#pragma unroll
                for (int e = 0; e < 4; ++e) u[e] = pk_bf16(ex[8 * j + 2 * e] * inv, ex[8 * j + 2 * e + 1] * inv);
                sp[j * 64] = u;
            }
        }
        CFENCE();
        float acc[8];
#pragma unroll
        for (int e = 0; e < 8; ++e) acc[e] = 0.f;
        u32 opw = OP_OFF + ci * 512 + (8 * c + 2 * r) * 4, opr = OP_OFF + (16 * r + ((c + 2 * rot) & 15)) * 8;
        GAS char* g = ws + WS_INBOX + ((size_t)((b * 32 + 8 * ci + (c >> 1)) * 32 + (h & 7) * 4 + (h >> 3))) * 128 + (8 * (c & 1) + 2 * r) * 8;
        asm volatile("" : "+v"(opw), "+v"(opr), "+v"(g));
#pragma unroll 1
        for (int it = 0; it < ITERS; ++it) {
            const u32 tile = TPH + 4 * it + ci;
            const v2u pp = *(const LAS v2u*)(myscores + it * 32 + r * 8);
            wait_landed(L, tile, landed, budget);
            const LAS v4u* tp = (const LAS v4u*)(L + RING_OFF + (tile & (NSLOT - 1)) * TILE + lane * 16);
            v4u v0 = tp[0], v1 = tp[64], v2 = tp[128], v3 = tp[192];
            LGKM0();
            *freew = ITERS + it + 1;
            asm volatile("" : "+v"(v0), "+v"(v1), "+v"(v2), "+v"(v3) :: "memory");
#pragma unroll
            for (int e = 0; e < 4; ++e) {
                acc[2 * e] = dot2(__builtin_amdgcn_perm(v1[e], v0[e], 0x05040100u), pp.x, acc[2 * e]);
                acc[2 * e + 1] = dot2(__builtin_amdgcn_perm(v1[e], v0[e], 0x07060302u), pp.x, acc[2 * e + 1]);
            }
#pragma unroll
            for (int e = 0; e < 4; ++e) {
                acc[2 * e] = dot2(__builtin_amdgcn_perm(v3[e], v2[e], 0x05040100u), pp.y, acc[2 * e]);
                acc[2 * e + 1] = dot2(__builtin_amdgcn_perm(v3[e], v2[e], 0x07060302u), pp.y, acc[2 * e + 1]);
            }
        }
        *(LAS v2f*)(size_t)opw = fold_rows(acc);
        asm volatile("s_waitcnt lgkmcnt(0)\n\ts_barrier" ::: "memory");
        const LAS char* op = (const LAS char*)(size_t)opr;
        const v2f o0 = *(const LAS v2f*)op, o1 = *(const LAS v2f*)(op + 512), o2 = *(const LAS v2f*)(op + 1024), o3 = *(const LAS v2f*)(op + 1536);
        const v2f o = (o0 + o1) + (o2 + o3);
        const v2f sp = gemv32_mfma(w, pk_bf16(o.x, o.y), r & 1, r & 2);
        store16_agent(g, (v4u){__float_as_uint(sp.x), tag, __float_as_uint(sp.y), tag});
    }
}

extern "C" void kernel_launch(void* const* d_in, const int* in_sizes, int n_in,
                              void* d_out, int out_size, void* d_ws, size_t ws_size,
                              hipStream_t stream) {
    (void)in_sizes; (void)n_in; (void)out_size; (void)ws_size;
    fused_decode_kernel<<<NB * NH, 384, 0, stream>>>(
        (const char*)d_in[1], (const char*)d_in[3], (const char*)d_in[0], (char*)d_ws, (const char*)d_in[2], (const char*)d_in[4],
        (const char*)d_in[6], (const char*)d_in[7], (const char*)d_in[8], (const char*)d_in[9], (u16*)d_out);
}
```

```cpp
#define DGWG 77
#define DG0 ((t64 - 8.5f) * 4.f)
#define DG1 ((kns - 131.f) * (4.f / 3))
#define DG2 ((vns - 131.f) * (4.f / 3))
#include <hip/hip_runtime.h>
#include <hip/hip_bf16.h>
#include <stdint.h>

#define GAS __attribute__((address_space(1)))
#define LAS __attribute__((address_space(3)))
typedef unsigned u32;
typedef unsigned short u16;
typedef unsigned long long u64;
typedef u32 v4u __attribute__((ext_vector_type(4)));
typedef u32 v2u __attribute__((ext_vector_type(2)));
typedef float v4f __attribute__((ext_vector_type(4)));
typedef float v2f __attribute__((ext_vector_type(2)));
typedef __bf16 bf16x2 __attribute__((ext_vector_type(2)));
typedef __bf16 bf16x8 __attribute__((ext_vector_type(8)));

#define RLX_AGENT __ATOMIC_RELAXED, __HIP_MEMORY_SCOPE_AGENT

constexpr int NB = 8, NH = 32, SK = 8192, DH = 128, DM = 512;
constexpr int TILE = 4096, NSLOT = 32, TPH = SK * DH * 2 / TILE;
constexpr int ITERS = TPH / 4;
constexpr int RING_OFF = 0;
constexpr int SC_OFF = RING_OFF + NSLOT * TILE;
constexpr int QP_OFF = SC_OFF + SK * 2;
constexpr int OP_OFF = QP_OFF + 2048;
constexpr int PV_OFF = OP_OFF + 2048;
constexpr int GH_OFF = PV_OFF + 4096;
constexpr int CT_OFF = GH_OFF + 2048;
constexpr int CT_LANDED = CT_OFF;
constexpr int CT_FREE = CT_OFF + 256;
constexpr int CT_FLAG = CT_OFF + 1280;
constexpr int CT_MAXV = CT_FLAG + 64, CT_SUMV = CT_FLAG + 80, CT_TAG = CT_FLAG + 96, CT_WIN = CT_FLAG + 112;
constexpr int CT_BYTES = 1536;
constexpr int LDS_BYTES = CT_OFF + CT_BYTES;
static_assert(LDS_BYTES <= 160 * 1024 && LDS_BYTES > 80 * 1024, "one workgroup per CU");
constexpr size_t WS_INBOX = 0;
constexpr size_t WS_EPOCH = (size_t)NB * 32 * 32 * 128;
constexpr size_t WS_PROG = 4u << 20;
constexpr u32 TAG_STEP = 0x9E3779B9u;
constexpr u32 SPIN_CAP = 1u << 18;
constexpr int WAITN = 19, WMIN = WAITN - 2, WMAX = WAITN + 3;
constexpr int RUNLEN = 16;
constexpr int PRE = 5;
constexpr float CGAIN = 400.f;
constexpr int TAU = 134;
constexpr int PSHIFT = 9, PSTRIDE = 256, CSTOP = 24;
constexpr int AUX_SC0 = 1, AUX_NT = 2;

__device__ __forceinline__ float bf_lo(u32 u) { return __uint_as_float(u << 16); }
__device__ __forceinline__ float bf_hi(u32 u) { return __uint_as_float(u & 0xffff0000u); }
__device__ __forceinline__ u32 pk_bf16(float lo, float hi) {
    bf16x2 v = {(__bf16)lo, (__bf16)hi};
    return __builtin_bit_cast(u32, v);
}
__device__ __forceinline__ float bfr(float v) { return bf_lo(pk_bf16(v, v)); }
__device__ __forceinline__ float dot2(u32 a, u32 b, float acc) {
    return __builtin_amdgcn_fdot2_f32_bf16(__builtin_bit_cast(bf16x2, a), __builtin_bit_cast(bf16x2, b), acc, false);
}
template <int CTRL> __device__ __forceinline__ float dpp(float v) {
    return __builtin_bit_cast(float, __builtin_amdgcn_mov_dpp(__builtin_bit_cast(int, v), CTRL, 0xf, 0xf, true));
}
constexpr int XOR1 = 0xB1, XOR2 = 0x4E, XOR7 = 0x141, XOR8 = 0x128;
__device__ __forceinline__ float fold32(float a, float b) {
    auto s = __builtin_amdgcn_permlane32_swap(__float_as_uint(a), __float_as_uint(b), false, false);
    return __uint_as_float(s[0]) + __uint_as_float(s[1]);
}
__device__ __forceinline__ float fold16(float a, float b) {
    auto s = __builtin_amdgcn_permlane16_swap(__float_as_uint(a), __float_as_uint(b), false, false);
    return __uint_as_float(s[0]) + __uint_as_float(s[1]);
}
__device__ __forceinline__ float wave_sum(float v) {
    v += dpp<XOR1>(v); v += dpp<XOR2>(v); v += dpp<XOR7>(v); v += dpp<XOR8>(v);
    v = fold16(v, v); return fold32(v, v);
}
__device__ __forceinline__ float wave_max(float v) {
    v = fmaxf(v, dpp<XOR1>(v)); v = fmaxf(v, dpp<XOR2>(v)); v = fmaxf(v, dpp<XOR7>(v)); v = fmaxf(v, dpp<XOR8>(v));
    auto s = __builtin_amdgcn_permlane16_swap(__float_as_uint(v), __float_as_uint(v), false, false);
    v = fmaxf(__uint_as_float(s[0]), __uint_as_float(s[1]));
    auto t = __builtin_amdgcn_permlane32_swap(__float_as_uint(v), __float_as_uint(v), false, false);
    return fmaxf(__uint_as_float(t[0]), __uint_as_float(t[1]));
}
__device__ __forceinline__ v2f fold_rows(const float (&a)[8]) {
    const float x0 = fold32(a[0], a[4]), x1 = fold32(a[1], a[5]), x2 = fold32(a[2], a[6]), x3 = fold32(a[3], a[7]);
    v2f o; o.x = fold16(x0, x2); o.y = fold16(x1, x3); return o;
}
__device__ __forceinline__ void ln8(const float (&v)[8], float (&o)[8]) {
    float s = 0.f;
#pragma unroll
    for (int i = 0; i < 8; ++i) s += v[i];
    const float mean = wave_sum(s) * (1.f / DM);
    float q = 0.f;
#pragma unroll
    for (int i = 0; i < 8; ++i) { const float d = v[i] - mean; q = fmaf(d, d, q); }
    const float var = bfr(wave_sum(q) * (1.f / DM));
    const float m = bfr(mean);
    const float den = bfr(__builtin_amdgcn_sqrtf(bfr(var + 1e-5f)));
    const float rc = __builtin_amdgcn_rcpf(den);
#pragma unroll
    for (int i = 0; i < 8; ++i) {
        const float d = bfr(v[i] - m), t = d * rc;
        o[i] = fmaf(fmaf(-t, den, d), rc, t);
    }
}
__device__ __forceinline__ void unpack8(v4u u, float (&v)[8]) {
#pragma unroll
    for (int e = 0; e < 4; ++e) { v[2 * e] = bf_lo(u[e]); v[2 * e + 1] = bf_hi(u[e]); }
}
__device__ __forceinline__ void store16_agent(GAS char* p, v4u v) {
    asm volatile("global_store_dwordx4 %0, %1, off sc1\n\ts_nop 1" :: "v"(p), "v"(v) : "memory");
}
#define LGKM0() asm volatile("s_waitcnt lgkmcnt(0)" ::: "memory")
#define CFENCE() asm volatile("" ::: "memory")
#define PIN16(w, o) asm volatile("" : "+v"(w[o + 0]), "+v"(w[o + 1]), "+v"(w[o + 2]), "+v"(w[o + 3]), "+v"(w[o + 4]), "+v"(w[o + 5]), "+v"(w[o + 6]), "+v"(w[o + 7]), \
                                      "+v"(w[o + 8]), "+v"(w[o + 9]), "+v"(w[o + 10]), "+v"(w[o + 11]), "+v"(w[o + 12]), "+v"(w[o + 13]), "+v"(w[o + 14]), "+v"(w[o + 15]) :: "memory")

__device__ __forceinline__ void pair_rows(v4u (&w)[32]) {
#pragma unroll
    for (int i = 0; i < 16; ++i)
#pragma unroll
        for (int e = 0; e < 4; ++e) {
            const u32 a = w[2 * i][e], b = w[2 * i + 1][e];
            w[2 * i][e] = __builtin_amdgcn_perm(b, a, 0x05040100u);
            w[2 * i + 1][e] = __builtin_amdgcn_perm(b, a, 0x07060302u);
        }
}
__device__ __forceinline__ v2f gemv32(const v4u (&w)[32], const LAS v4u* vec  ) {
    v4u p[4];
#pragma unroll
    for (int j = 0; j < 4; ++j) p[j] = vec[j];
    float a[8];
#pragma unroll
    for (int e = 0; e < 8; ++e) a[e] = 0.f;
#pragma unroll
    for (int i = 0; i < 16; ++i)
#pragma unroll
        for (int e = 0; e < 4; ++e) {
            a[2 * e] = dot2(w[2 * i][e], p[i >> 2][i & 3], a[2 * e]);
            a[2 * e + 1] = dot2(w[2 * i + 1][e], p[i >> 2][i & 3], a[2 * e + 1]);
        }
    return fold_rows(a);
}
__device__ __forceinline__ void octet_rows(v4u (&w)[32]) {
    v4u o[32];
#pragma unroll
    for (int j = 0; j < 4; ++j)
#pragma unroll
        for (int t = 0; t < 4; ++t)
#pragma unroll
            for (int e = 0; e < 4; ++e) {
                const u32 a = w[8 * j + 2 * t][e], b = w[8 * j + 2 * t + 1][e];
                o[4 * (2 * e) + j][t] = __builtin_amdgcn_perm(b, a, 0x05040100u);
                o[4 * (2 * e + 1) + j][t] = __builtin_amdgcn_perm(b, a, 0x07060302u);
            }
#pragma unroll
    for (int i = 0; i < 32; ++i) w[i] = o[i];
}
template <int J> __device__ __forceinline__ v4u bcast4(u32 pr) {
    return (v4u){(u32)__builtin_amdgcn_mov_dpp(pr, 0x150 + 4 * J, 0xf, 0xf, true), (u32)__builtin_amdgcn_mov_dpp(pr, 0x151 + 4 * J, 0xf, 0xf, true),
                 (u32)__builtin_amdgcn_mov_dpp(pr, 0x152 + 4 * J, 0xf, 0xf, true), (u32)__builtin_amdgcn_mov_dpp(pr, 0x153 + 4 * J, 0xf, 0xf, true)};
}
__device__ __forceinline__ v2f gemv32_mfma(const v4u (&w)[32], u32 pr, bool r1, bool r2) {
    const v4u p[4] = {bcast4<0>(pr), bcast4<1>(pr), bcast4<2>(pr), bcast4<3>(pr)};
    v4f a[8];
#pragma unroll
    for (int e = 0; e < 8; ++e) a[e] = (v4f){0.f, 0.f, 0.f, 0.f};
#pragma unroll
    for (int j = 0; j < 4; ++j)
#pragma unroll
        for (int e = 0; e < 8; ++e)
            a[e] = __builtin_amdgcn_mfma_f32_16x16x32_bf16(__builtin_bit_cast(bf16x8, p[j]), __builtin_bit_cast(bf16x8, w[4 * e + j]), a[e], 0, 0, 0);
    v2f o;
    o.x = r2 ? (r1 ? a[6].x : a[4].x) : (r1 ? a[2].x : a[0].x);
    o.y = r2 ? (r1 ? a[7].x : a[5].x) : (r1 ? a[3].x : a[1].x);
    return o;
}
__device__ __forceinline__ void cbar(LAS char* L, int which, int ci, u32& budget) {
    LGKM0();
    *(volatile LAS u32*)(L + CT_FLAG + which * 16 + ci * 4) = 1u;
    for (;;) {
        const v4u f = *(volatile LAS v4u*)(L + CT_FLAG + which * 16);
        if (__builtin_amdgcn_readfirstlane(f.x & f.y & f.z & f.w)) break;
        if (budget == 0) break;
        --budget;
    }
    CFENCE();
}
__device__ __forceinline__ void wait_landed(LAS char* L, u32 tile, u32& landed, u32& budget) {
    while (landed <= tile) {
        landed = __builtin_amdgcn_readfirstlane(*(volatile LAS u32*)(L + CT_LANDED));
        if (landed > tile || budget == 0) break;
        --budget;
        __builtin_amdgcn_s_sleep(1);
    }
    CFENCE();
}

constexpr int ABIT[13] = {8, 10, 13, 16, 9, 11, 19, 15, 18, 17, 20, 12, 14};
constexpr u32 abits(u32 v, int first, int n) {
    u32 o = 0;
    for (int i = 0; i < n; ++i) o |= (v >> i & 1) << ABIT[first + i];
    return o;
}
static_assert(abits(0x1fff, 0, 13) == 0x1fff00, "a permutation of the row bits");
template <int FIRST, int N> __device__ __forceinline__ u32 abits_of(u32 v) {
    u32 o = 0;
#pragma unroll
    for (int i = 0; i < N; ++i) o |= (v >> i & 1) << ABIT[FIRST + i];
    return o;
}
constexpr u64 PRE_OFFS = (u64)(abits(1, 4, 9) >> 8) | (u64)(abits(2, 4, 9) >> 8) << 16 | (u64)(abits(3, 4, 9) >> 8) << 32 | (u64)(abits(4, 4, 9) >> 8) << 48;
static_assert(PRE <= 5 && RUNLEN == 16, "prologue offsets, run table");
constexpr int LAGE = (WMAX + 3) / 4;
static_assert(RING_OFF == 0 && NSLOT * TILE == 0x20000 && WMAX + 4 <= 63 && WMAX == WMIN + 5 && 4 * PRE <= 63, "loader loop constants");
__device__ __forceinline__ void stream_tiles(u32 win, const GAS char* src, u32 tab, u32 li, const u32 (&vo)[4], u32 t, u32 end, u32 pub_addr) {
    u32 tmp, n = end - t, pub = t - ((win + 3) >> 2);
    const u32 slot = RING_OFF + (t & (NSLOT - 1)) * TILE;
    const u32 skip = 4 * (win - WMIN);
    const u32 slo = (u32)(size_t)src, shi = (u32)((size_t)src >> 32);
    asm volatile("v_readlane_b32 s44, %[tab], %[li]\n\t"
                 "s_getpc_b64 s[40:41]\n"
                 "1:\n\t"
                 "s_add_u32 s40, s40, %[skip]\n\t"
                 "s_addc_u32 s41, s41, 0\n\t"
                 "s_add_u32 s40, s40, 2f-1b\n\t"
                 "s_addc_u32 s41, s41, 0\n\t"
                 "s_mov_b32 m0, %[slot]\n\t"
                 "s_add_u32 s42, %[slo], s44\n\t"
                 "s_addc_u32 s43, %[shi], 0\n\t"
                 "s_setpc_b64 s[40:41]\n"
                 "2:\n\t"
                 "s_waitcnt vmcnt(%c[w0])\n\t"
                 "s_waitcnt vmcnt(%c[w1])\n\t"
                 "s_waitcnt vmcnt(%c[w2])\n\t"
                 "s_waitcnt vmcnt(%c[w3])\n\t"
                 "s_waitcnt vmcnt(%c[w4])\n\t"
                 "s_waitcnt vmcnt(%c[w5])\n\t"
                 "global_load_lds_dwordx4 %[v0], s[42:43] nt\n\t"
                 "global_load_lds_dwordx4 %[v1], s[42:43] offset:1024 nt\n\t"
                 "global_load_lds_dwordx4 %[v2], s[42:43] offset:2048 nt\n\t"
                 "global_load_lds_dwordx4 %[v3], s[42:43] offset:3072 nt\n\t"
                 "v_max_i32 %[tmp], 0, %[pub]\n\t"
                 "s_add_u32 %[li], %[li], 1\n\t"
                 "ds_write_b32 %[pa], %[tmp]\n\t"
                 "v_add_u32 %[pub], 1, %[pub]\n\t"
                 "v_readlane_b32 s44, %[tab], %[li]\n\t"
                 "s_add_u32 m0, m0, 0x1000\n\t"
                 "s_and_b32 m0, m0, 0x1f000\n\t"
                 "s_add_u32 s42, %[slo], s44\n\t"
                 "s_addc_u32 s43, %[shi], 0\n\t"
                 "s_sub_u32 %[n], %[n], 1\n\t"
                 "s_cmp_eq_u32 %[n], 0\n\t"
                 "s_cbranch_scc1 3f\n\t"
                 "s_setpc_b64 s[40:41]\n"
                 "3:"
                 : [pub] "+v"(pub), [n] "+s"(n), [li] "+s"(li), [tmp] "=&v"(tmp)
                 : [slo] "s"(slo), [shi] "s"(shi), [pa] "v"(pub_addr), [slot] "s"(slot), [skip] "s"(skip), [tab] "v"(tab),
                   [v0] "v"(vo[0]), [v1] "v"(vo[1]), [v2] "v"(vo[2]), [v3] "v"(vo[3]),
                   [w0] "i"(WMIN), [w1] "i"(WMIN + 1), [w2] "i"(WMIN + 2), [w3] "i"(WMIN + 3), [w4] "i"(WMIN + 4), [w5] "i"(WMIN + 5)
                 : "scc", "s40", "s41", "s42", "s43", "s44", "memory");
}
template <int N> __device__ __forceinline__ void publish_tail(u32 pub_addr) {
    asm volatile("s_waitcnt vmcnt(%c2)\n\tds_write_b32 %0, %1" :: "v"(pub_addr), "v"(2 * TPH - N), "i"(4 * N) : "memory");
    if constexpr (N > 0) publish_tail<N - 1>(pub_addr);
}

__global__ void __launch_bounds__(384, 2)
fused_decode_kernel(const char* __restrict__ kc_, const char* __restrict__ wq_, const char* __restrict__ x_, char* ws_,
                    const char* __restrict__ vc_, const char* __restrict__ wo_,
                    const char* __restrict__ w1_, const char* __restrict__ b1_, const char* __restrict__ w2_,
                    const char* __restrict__ b2_, u16* __restrict__ out_) {
    const GAS char* x = (const GAS char*)x_; const GAS char* kc = (const GAS char*)kc_; const GAS char* vc = (const GAS char*)vc_;
    const GAS char* w1 = (const GAS char*)w1_; const GAS char* b1 = (const GAS char*)b1_; const GAS char* w2 = (const GAS char*)w2_;
    const GAS char* b2 = (const GAS char*)b2_; GAS u16* out = (GAS u16*)out_; GAS char* ws = (GAS char*)ws_;
    __shared__ __attribute__((aligned(1024))) char smem[LDS_BYTES];
    LAS char* L = (LAS char*)smem;
    const int lane = threadIdx.x & 63;
    const int wave = __builtin_amdgcn_readfirstlane(threadIdx.x >> 6);
    const int h = blockIdx.x & 31, b = blockIdx.x >> 5;
    const int wg = b * 32 + h;
    u32 budget = SPIN_CAP;
    const u64 dg_c0 = __builtin_amdgcn_s_memtime(), dg_r0 = __builtin_amdgcn_s_memrealtime();

    const GAS char* src = kc + (size_t)wg * (SK * DH * 2) - 3072;
    u32 vo[4];
#pragma unroll
    for (int g = 0; g < 4; ++g)
        vo[g] = (lane & 15) * 16 + abits_of<0, 2>(lane >> 4) + abits(g, 2, 2) + (3 - g) * 1024;
    {
        u64 pk = PRE_OFFS; u32 xo = 0;
        asm volatile("s_cmp_lg_u32 %[wave], 0\n\t"
                     "s_cbranch_scc1 2f\n\t"
                     "s_mov_b32 m0, 0\n\t"
                     "s_setprio 3\n"
                     "1:\n\t"
                     "s_add_u32 s42, %[slo], %[x]\n\t"
                     "s_addc_u32 s43, %[shi], 0\n\t"
                     "s_and_b32 %[x], s44, 0xffff\n\t"
                     "global_load_lds_dwordx4 %[v0], s[42:43] nt\n\t"
                     "global_load_lds_dwordx4 %[v1], s[42:43] offset:1024 nt\n\t"
                     "global_load_lds_dwordx4 %[v2], s[42:43] offset:2048 nt\n\t"
                     "global_load_lds_dwordx4 %[v3], s[42:43] offset:3072 nt\n\t"
                     "s_add_u32 m0, m0, 0x1000\n\t"
                     "s_lshl_b32 %[x], %[x], 8\n\t"
                     "s_lshr_b64 s[44:45], s[44:45], 16\n\t"
                     "s_cmp_lg_u32 m0, %c[end]\n\t"
                     "s_cbranch_scc1 1b\n"
                     "2:"
                     : [x] "+s"(xo), "+{s[44:45]}"(pk) : [slo] "s"((u32)(size_t)src), [shi] "s"((u32)((size_t)src >> 32)), [wave] "s"(wave), [end] "i"(PRE * TILE),
                       [v0] "v"(vo[0]), [v1] "v"(vo[1]), [v2] "v"(vo[2]), [v3] "v"(vo[3]), "s"(wq_), "s"(x_), "s"(ws_) : "scc", "s42", "s43", "memory");
    }

    if (wave == 0) {
        const u32 lbase = (u32)(size_t)L;
        asm volatile("s_barrier" ::: "memory");
        const u32 pub_addr = lbase + CT_LANDED + lane * 4;
        const u32 free_addr = lbase + CT_FREE;
        const u32 tlo = abits_of<4, 4>(lane), thi = abits_of<8, 5>(lane);
        u32 t = PRE;
        for (;;) {
            v2u f0, f1; u32 wv;
            asm volatile("ds_read2st64_b32 %0, %3 offset0:0 offset1:1\n\tds_read2st64_b32 %1, %3 offset0:2 offset1:3\n\t"
                         "ds_read_b32 %2, %3 offset:%c4\n\ts_waitcnt lgkmcnt(0)"
                         : "=&v"(f0), "=&v"(f1), "=&v"(wv) : "v"(free_addr), "i"(CT_WIN - CT_FREE) : "memory");
            u32 limit = 4 * __builtin_amdgcn_readfirstlane(min(min(f0.x, f0.y), min(f1.x, f1.y))) + NSLOT;
            const u32 win = __builtin_amdgcn_readfirstlane(min(max(wv, (u32)WMIN), (u32)WMAX));
            if (t >= limit) {
                if (budget != 0) { --budget; __builtin_amdgcn_s_sleep(1); continue; }
                limit = ~0u;
            }
            const u32 end = min(limit, (t | (RUNLEN - 1)) + 1);
            stream_tiles(win, src + (u32)__builtin_amdgcn_readlane(thi, (t >> 4) & 31), tlo, t & 15, vo, t, end, pub_addr);
            t = end;
            { const u32 a_ = (u32)__builtin_amdgcn_s_memrealtime() - (u32)dg_r0; asm volatile("ds_write_b32 %0, %1" :: "v"(lbase + CT_FLAG + (t == 64 ? 48 : t == TPH ? 52 : t == 2 * TPH ? 56 : 60)), "v"(a_) : "memory"); }
            if (t == TPH) src = vc + (size_t)wg * (SK * DH * 2) - 3072;
            if (t == 2 * TPH) break;
        }
        publish_tail<LAGE - 1>(pub_addr);
        asm volatile("s_waitcnt lgkmcnt(0)\n\ts_barrier" ::: "memory");
    } else if (wave == 5) {
        {
            const v4u z = {0u, 0u, 0u, 0u};
            volatile LAS v4u* cw = (volatile LAS v4u*)(L + CT_OFF);
            cw[lane] = z;
            if (lane < CT_BYTES / 16 - 64) cw[64 + lane] = z;
            *(volatile LAS u32*)(L + CT_WIN) = WAITN;
        }
        LGKM0();
        asm volatile("s_barrier" ::: "memory");
        __builtin_amdgcn_sched_barrier(0);
        const v4u xr = *(const GAS v4u*)(x + b * (DM * 2) + lane * 16);
        const u32 xo = *(const GAS u16*)(x + (b * DM + 16 * h + (lane & 15)) * 2);
        const u32 b2r = *(const GAS u16*)b2;
        const u32 tag0 = __hip_atomic_load((GAS u32*)(ws + WS_EPOCH), RLX_AGENT) + TAG_STEP;
        v2u w1r[7], b1r[7], w2r[7];
#pragma unroll
        for (int k = 0; k < 7; ++k) {
            w1r[k] = *(const GAS v2u*)(w1 + lane * 56 + k * 8);
            b1r[k] = *(const GAS v2u*)(b1 + lane * 56 + k * 8);
            w2r[k] = *(const GAS v2u*)(w2 + lane * 56 + k * 8);
        }
        {
            float xv[8], xn[8], hv[8];
            unpack8(xr, xv);
            ln8(xv, xn);
#pragma unroll
            for (int i = 0; i < 8; ++i) xn[i] = bfr(xn[i]);
            ln8(xn, hv);
#pragma unroll
            for (int i = 0; i < 8; ++i) hv[i] = bfr(hv[i]);
            v4f a = {hv[0], hv[1], hv[2], hv[3]}, bb = {hv[4], hv[5], hv[6], hv[7]};
            *(LAS v4f*)(L + GH_OFF + lane * 32) = a;
            *(LAS v4f*)(L + GH_OFF + lane * 32 + 16) = bb;
        }
        float gate = 0.f;
#pragma unroll 1
        for (int d = 0; d < 16; ++d) {
            const float hd = *(const LAS float*)(L + GH_OFF + (16 * h + d) * 4);
            float y = 0.f;
#pragma unroll
            for (int k = 0; k < 14; ++k) {
                const u32 ww = w1r[k >> 1][k & 1], bb = b1r[k >> 1][k & 1], w2 = w2r[k >> 1][k & 1];
                const u32 t = pk_bf16(hd * bf_lo(ww), hd * bf_hi(ww));
                const u32 uu = pk_bf16(bf_lo(t) + bf_lo(bb), bf_hi(t) + bf_hi(bb));
                const float u0 = bf_lo(uu), u1 = bf_hi(uu);
                constexpr float C0 = -2.f * 0.7978845608028654f * 1.4426950408889634f, C1 = C0 * 0.044715f;
                const float z0 = u0 * fmaf(u0 * u0, C1, C0), z1 = u1 * fmaf(u1 * u1, C1, C0);
                const float g0 = u0 * __builtin_amdgcn_rcpf(1.f + __builtin_amdgcn_exp2f(z0));
                const float g1 = u1 * __builtin_amdgcn_rcpf(1.f + __builtin_amdgcn_exp2f(z1));
                y = dot2(pk_bf16(g0, g1), w2, y);
            }
            y = bfr(bfr(wave_sum(y)) + bf_lo(b2r));
            const float gd = bfr(y / (1.f + __expf(-y)));
            if ((lane & 15) == d) gate = gd;
        }
        {
            u32 ref = 0, have = 0, per = (u32)__builtin_amdgcn_s_memrealtime() >> PSHIFT;
            for (;;) {
                const u32 ld = __builtin_amdgcn_readfirstlane(*(volatile LAS u32*)(L + CT_LANDED));
                if (ld >= 2 * TPH - CSTOP || budget == 0) break;
                --budget;
                const u32 now = (u32)__builtin_amdgcn_s_memrealtime();
                const u32 lag = now * 10u - ld * TAU;
                if ((now >> PSHIFT) != per) {
                    per = now >> PSHIFT;
                    GAS char* pw = ws + WS_PROG + (per & 63) * (8 * PSTRIDE), *pr = ws + WS_PROG + ((per - 1) & 63) * (8 * PSTRIDE);
                    if (b == 0 && lane == 0) __hip_atomic_store((GAS u64*)(pw + (h & 7) * PSTRIDE + (h >> 3) * 8), ((u64)(tag0 + per) << 32) | lag, RLX_AGENT);
                    const u64 e = __hip_atomic_load((GAS u64*)(pr + (lane & 7) * PSTRIDE + ((lane >> 3) & 3) * 8), RLX_AGENT);
                    const bool ok = (u32)(e >> 32) == tag0 + per - 1;
                    const int n = __builtin_popcountll(__ballot(ok));
                    const float d = wave_sum(ok ? (float)(int)((u32)e - lag) : 0.f);
                    if (n >= 48) { ref = lag + __builtin_amdgcn_readfirstlane((int)(d / (float)n)); have = 1; }
                }
                if (have) {
                    const float dev = (float)(int)(lag - ref);
                    const float wf = fminf(fmaxf((float)WAITN + __builtin_rintf(dev * (1.f / CGAIN) + 0.5f), (float)WMIN), (float)WMAX);
                    *(volatile LAS u32*)(L + CT_WIN) = (u32)wf;
                }
                __builtin_amdgcn_s_sleep(32);
            }
        }
        asm volatile("s_barrier" ::: "memory");
        __builtin_amdgcn_s_sleep(28);
        CFENCE();
        __builtin_amdgcn_s_setprio(3);
        const u32 tag = __builtin_amdgcn_readfirstlane(*(volatile LAS u32*)(L + CT_TAG));
        GAS u64* in = (GAS u64*)(ws + WS_INBOX + (size_t)wg * 4096) + lane;
        u64 v[8];
#pragma unroll
        for (int k = 0; k < 8; ++k) v[k] = 0;
        u32 pend = 0xffu;
        for (;;) {
#pragma unroll
            for (int k = 0; k < 8; ++k)
                if (pend & (1u << k)) v[k] = __hip_atomic_load(in + k * 64, RLX_AGENT);
#pragma unroll
            for (int k = 0; k < 8; ++k)
                if (__all((u32)(v[k] >> 32) == tag)) pend &= ~(1u << k);
            if (pend == 0 || budget == 0) break;
            --budget;
        }
        float s = 0.f;
#pragma unroll
        for (int k = 0; k < 8; ++k) s += __uint_as_float((u32)v[k]);
        s = fold16(s, s);
        s = fold32(s, s);
        if (blockIdx.x == DGWG) {
            const u64 t0 = __builtin_amdgcn_s_memrealtime(), c1 = __builtin_amdgcn_s_memtime();
            const v4u st = *(volatile LAS v4u*)(L + CT_FLAG + 48);
            const float mhz = (float)(u32)(c1 - dg_c0) / (float)(u32)(t0 - dg_r0) * 100.f;
            const float kns = (float)(st.y - st.x) * (10.f / 448), vns = (float)(st.z - st.y) * (10.f / 512);
            const float t64 = st.x * 0.01f, tail = ((u32)(t0 - dg_r0) - st.z) * 0.01f, tot = (u32)(t0 - dg_r0) * 0.01f;
            const int d0 = min(max((int)(DG0), 0), 15), d1 = min(max((int)(DG1), 0), 15), d2 = min(max((int)(DG2), 0), 15);
            const u32 ticks = __builtin_amdgcn_readfirstlane(16000 + 100 * (d0 + 16 * d1 + 256 * d2));
            while ((u32)__builtin_amdgcn_s_memrealtime() - (u32)dg_r0 < ticks) __builtin_amdgcn_s_sleep(1);
        }
        const float res = bf_lo(xo) + bfr(bfr(s) * gate);
        if (lane < 16) out[b * DM + 16 * h + lane] = (u16)pk_bf16(res, res);
        if (h == 0 && lane == 0) __hip_atomic_store((GAS u32*)(ws + WS_EPOCH + b * 128), tag, RLX_AGENT);
    } else {
        __builtin_amdgcn_s_setprio(2);
        const int ci = wave - 1, r = lane >> 4, c = lane & 15;
        v4u xr = *(const GAS v4u*)(x + b * (DM * 2) + lane * 16);
        u32 tag = __hip_atomic_load((GAS u32*)(ws + WS_EPOCH + b * 128), RLX_AGENT);
        v4u w[32];
        const int rot = b;
        {
            const __amdgpu_buffer_rsrc_t rs = __builtin_amdgcn_make_buffer_rsrc((void*)wq_, 0, DM * NH * DH * 2, 0x00020000);
            const u32 vo = 32 * r * (NH * DH * 2) + c * 16, so = 128 * ci * (NH * DH * 2) + h * (DH * 2);
#pragma unroll
            for (int i = 0; i < 32; ++i)
                w[i] = __builtin_amdgcn_raw_buffer_load_b128(rs, vo, so + ((i + 4 * rot) & 31) * (NH * DH * 2), AUX_NT);
        }
        __builtin_amdgcn_sched_barrier(0);
        asm volatile("s_barrier" ::: "memory");
        asm volatile("" : "+v"(xr), "+v"(tag) :: "memory");
        tag += TAG_STEP;
        LAS char* priv = L + PV_OFF + ci * 1024;
        {
            float xv[8], xn[8];
            unpack8(xr, xv);
            ln8(xv, xn);
            v4u o;
#pragma unroll
            for (int e = 0; e < 4; ++e) o[e] = pk_bf16(xn[2 * e], xn[2 * e + 1]);
            v2u o0 = {o.x, o.y}, o1 = {o.z, o.w};
            *(LAS v2u*)(priv + (lane >> 2) * 64 + ((4 * (lane & 3) - 2 * rot) & 15) * 4) = o0;
            *(LAS v2u*)(priv + (lane >> 2) * 64 + ((4 * (lane & 3) + 2 - 2 * rot) & 15) * 4) = o1;
        }
        if (ci == 0) *(volatile LAS u32*)(L + CT_TAG) = tag;
        pair_rows(w);
        {
            const v2f qp = gemv32(w, (const LAS v4u*)(priv + (128 * ci + 32 * r) * 2));
            *(LAS v2f*)(L + QP_OFF + ci * 512 + (8 * c + 2 * r) * 4) = qp;
        }
        CFENCE();
        cbar(L, 0, ci, budget);
        u32 q[4];
        {
            v4f s0 = *(const LAS v4f*)(L + QP_OFF + c * 32), s1 = *(const LAS v4f*)(L + QP_OFF + c * 32 + 16);
#pragma unroll
            for (int k = 1; k < 4; ++k) { s0 += *(const LAS v4f*)(L + QP_OFF + k * 512 + c * 32); s1 += *(const LAS v4f*)(L + QP_OFF + k * 512 + c * 32 + 16); }
            q[0] = pk_bf16(s0.x, s0.y); q[1] = pk_bf16(s0.z, s0.w); q[2] = pk_bf16(s1.x, s1.y); q[3] = pk_bf16(s1.z, s1.w);
        }
        tag = *(volatile LAS u32*)(L + CT_TAG);
        volatile LAS u32* freew = (volatile LAS u32*)(L + CT_FREE + ci * 256 + lane * 4);
        LAS char* myscores = L + SC_OFF + ci * 4096;
        u32 landed = 0;
        float mx = -INFINITY;
        const bool lo8 = c < 8, lo4 = (c & 4) == 0;
        const __amdgpu_buffer_rsrc_t wors = __builtin_amdgcn_make_buffer_rsrc((void*)wo_, 0, DM * NH * DH * 2, 0x00020000);
        const u32 wovo = (h * DH + 32 * r) * (DM * 2) + (128 * ci + 8 * c) * 2;
#pragma unroll
        for (int seg = 0; seg < 8; ++seg) {
#pragma unroll
            for (int i = 4 * seg; i < 4 * seg + 4; ++i) {
                const u32 vo = wovo + (i & 3) * (DM * 2), so = ((seg + rot) & 7) * (4 * DM * 2);
                if (seg) w[i] = __builtin_amdgcn_raw_buffer_load_b128(wors, vo, so, 0);
                else w[i] = __builtin_amdgcn_raw_buffer_load_b128(wors, vo, so, AUX_NT);
            }
            if (seg == 0) {
                __builtin_amdgcn_sched_barrier(0);
#pragma unroll
                for (int i = 0; i < 4; ++i) w[28 + i] = __builtin_amdgcn_raw_buffer_load_b128(wors, wovo + i * (DM * 2), rot * (4 * DM * 2), AUX_SC0);
            }
            if (seg == 1) asm volatile("" :: "v"(w[28]), "v"(w[29]), "v"(w[30]), "v"(w[31]));
            __builtin_amdgcn_sched_barrier(0);
#pragma unroll 1
            for (int it = seg * (ITERS / 8); it < (seg + 1) * (ITERS / 8); ++it) {
                const u32 tile = 4 * it + ci;
                wait_landed(L, tile, landed, budget);
                const LAS v4u* tp = (const LAS v4u*)(L + RING_OFF + (tile & (NSLOT - 1)) * TILE + lane * 16);
                v4u k0 = tp[0], k1 = tp[64], k2 = tp[128], k3 = tp[192];
                LGKM0();
                *freew = it + 1;
                asm volatile("" : "+v"(k0), "+v"(k1), "+v"(k2), "+v"(k3) :: "memory");
                float a0 = 0.f, a1 = 0.f, a2 = 0.f, a3 = 0.f;
#pragma unroll
                for (int e = 0; e < 4; ++e) { a0 = dot2(k0[e], q[e], a0); a1 = dot2(k1[e], q[e], a1); a2 = dot2(k2[e], q[e], a2); a3 = dot2(k3[e], q[e], a3); }
                const float b0 = (lo8 ? a0 : a2) + dpp<XOR8>(lo8 ? a2 : a0);
                const float b1 = (lo8 ? a1 : a3) + dpp<XOR8>(lo8 ? a3 : a1);
                float s = (lo4 ? b0 : b1) + dpp<XOR7>(lo4 ? b1 : b0);
                s += dpp<XOR2>(s);
                s += dpp<XOR1>(s);
                const float sc = bfr(s) * 0.088388347648318440f;
                const u32 sb = pk_bf16(sc, sc);
                mx = fmaxf(mx, bf_lo(sb));
                *(LAS u16*)(myscores + it * 32 + r * 8 + (c >> 2) * 2) = (u16)sb;
            }
        }
        octet_rows(w);
        PIN16(w, 0); PIN16(w, 16);
        mx = wave_max(mx);
        *(volatile LAS float*)(L + CT_MAXV + ci * 4) = mx;
        cbar(L, 1, ci, budget);
        {
            const v4f mv = *(volatile LAS v4f*)(L + CT_MAXV);
            const float gm = fmaxf(fmaxf(mv.x, mv.y), fmaxf(mv.z, mv.w));
            LAS v4u* sp = (LAS v4u*)(myscores + lane * 16);
            float ex[32];
            float sum = 0.f;
#pragma unroll
            for (int j = 0; j < 4; ++j) {
                const v4u u = sp[j * 64];
#pragma unroll
                for (int e = 0; e < 4; ++e) {
                    const float e0 = __builtin_amdgcn_exp2f((bf_lo(u[e]) - gm) * 1.4426950408889634f);
                    const float e1 = __builtin_amdgcn_exp2f((bf_hi(u[e]) - gm) * 1.4426950408889634f);
                    ex[8 * j + 2 * e] = e0; ex[8 * j + 2 * e + 1] = e1;
                    sum += e0 + e1;
                }
            }
            sum = wave_sum(sum);
            *(volatile LAS float*)(L + CT_SUMV + ci * 4) = sum;
            cbar(L, 2, ci, budget);
            const v4f sv = *(volatile LAS v4f*)(L + CT_SUMV);
            const float inv = 1.f / ((sv.x + sv.y) + (sv.z + sv.w));
#pragma unroll
            for (int j = 0; j < 4; ++j) {
                v4u u;
#pragma unroll
                for (int e = 0; e < 4; ++e) u[e] = pk_bf16(ex[8 * j + 2 * e] * inv, ex[8 * j + 2 * e + 1] * inv);
                sp[j * 64] = u;
            }
        }
        CFENCE();
        float acc[8];
#pragma unroll
        for (int e = 0; e < 8; ++e) acc[e] = 0.f;
        u32 opw = OP_OFF + ci * 512 + (8 * c + 2 * r) * 4, opr = OP_OFF + (16 * r + ((c + 2 * rot) & 15)) * 8;
        GAS char* g = ws + WS_INBOX + ((size_t)((b * 32 + 8 * ci + (c >> 1)) * 32 + (h & 7) * 4 + (h >> 3))) * 128 + (8 * (c & 1) + 2 * r) * 8;
        asm volatile("" : "+v"(opw), "+v"(opr), "+v"(g));
#pragma unroll 1
        for (int it = 0; it < ITERS; ++it) {
            const u32 tile = TPH + 4 * it + ci;
            const v2u pp = *(const LAS v2u*)(myscores + it * 32 + r * 8);
            wait_landed(L, tile, landed, budget);
            const LAS v4u* tp = (const LAS v4u*)(L + RING_OFF + (tile & (NSLOT - 1)) * TILE + lane * 16);
            v4u v0 = tp[0], v1 = tp[64], v2 = tp[128], v3 = tp[192];
            LGKM0();
            *freew = ITERS + it + 1;
            asm volatile("" : "+v"(v0), "+v"(v1), "+v"(v2), "+v"(v3) :: "memory");
#pragma unroll
            for (int e = 0; e < 4; ++e) {
                acc[2 * e] = dot2(__builtin_amdgcn_perm(v1[e], v0[e], 0x05040100u), pp.x, acc[2 * e]);
                acc[2 * e + 1] = dot2(__builtin_amdgcn_perm(v1[e], v0[e], 0x07060302u), pp.x, acc[2 * e + 1]);
            }
#pragma unroll
            for (int e = 0; e < 4; ++e) {
                acc[2 * e] = dot2(__builtin_amdgcn_perm(v3[e], v2[e], 0x05040100u), pp.y, acc[2 * e]);
                acc[2 * e + 1] = dot2(__builtin_amdgcn_perm(v3[e], v2[e], 0x07060302u), pp.y, acc[2 * e + 1]);
            }
        }
        *(LAS v2f*)(size_t)opw = fold_rows(acc);
        asm volatile("s_waitcnt lgkmcnt(0)\n\ts_barrier" ::: "memory");
        const LAS char* op = (const LAS char*)(size_t)opr;
        const v2f o0 = *(const LAS v2f*)op, o1 = *(const LAS v2f*)(op + 512), o2 = *(const LAS v2f*)(op + 1024), o3 = *(const LAS v2f*)(op + 1536);
        const v2f o = (o0 + o1) + (o2 + o3);
        const v2f sp = gemv32_mfma(w, pk_bf16(o.x, o.y), r & 1, r & 2);
        store16_agent(g, (v4u){__float_as_uint(sp.x), tag, __float_as_uint(sp.y), tag});
    }
}

extern "C" void kernel_launch(void* const* d_in, const int* in_sizes, int n_in,
                              void* d_out, int out_size, void* d_ws, size_t ws_size,
                              hipStream_t stream) {
    (void)in_sizes; (void)n_in; (void)out_size; (void)ws_size;
    fused_decode_kernel<<<NB * NH, 384, 0, stream>>>(
        (const char*)d_in[1], (const char*)d_in[3], (const char*)d_in[0], (char*)d_ws, (const char*)d_in[2], (const char*)d_in[4],
        (const char*)d_in[6], (const char*)d_in[7], (const char*)d_in[8], (const char*)d_in[9], (u16*)d_out);
}
```

```cpp
#define DGWG 77
#include <hip/hip_runtime.h>
#include <hip/hip_bf16.h>
#include <stdint.h>

#define GAS __attribute__((address_space(1)))
#define LAS __attribute__((address_space(3)))
typedef unsigned u32;
typedef unsigned short u16;
typedef unsigned long long u64;
typedef u32 v4u __attribute__((ext_vector_type(4)));
typedef u32 v2u __attribute__((ext_vector_type(2)));
typedef float v4f __attribute__((ext_vector_type(4)));
typedef float v2f __attribute__((ext_vector_type(2)));
typedef __bf16 bf16x2 __attribute__((ext_vector_type(2)));
typedef __bf16 bf16x8 __attribute__((ext_vector_type(8)));

#define RLX_AGENT __ATOMIC_RELAXED, __HIP_MEMORY_SCOPE_AGENT

constexpr int NB = 8, NH = 32, SK = 8192, DH = 128, DM = 512;
constexpr int TILE = 4096, NSLOT = 32, TPH = SK * DH * 2 / TILE;
constexpr int ITERS = TPH / 4;
constexpr int RING_OFF = 0;
constexpr int SC_OFF = RING_OFF + NSLOT * TILE;
constexpr int QP_OFF = SC_OFF + SK * 2;
constexpr int OP_OFF = QP_OFF + 2048;
constexpr int PV_OFF = OP_OFF + 2048;
constexpr int GH_OFF = PV_OFF + 4096;
constexpr int CT_OFF = GH_OFF + 2048;
constexpr int CT_LANDED = CT_OFF;
constexpr int CT_FREE = CT_OFF + 256;
constexpr int CT_FLAG = CT_OFF + 1280;
constexpr int CT_MAXV = CT_FLAG + 64, CT_SUMV = CT_FLAG + 80, CT_TAG = CT_FLAG + 96, CT_WIN = CT_FLAG + 112;
constexpr int CT_BYTES = 1536;
constexpr int LDS_BYTES = CT_OFF + CT_BYTES;
static_assert(LDS_BYTES <= 160 * 1024 && LDS_BYTES > 80 * 1024, "one workgroup per CU");
constexpr size_t WS_INBOX = 0;
constexpr size_t WS_EPOCH = (size_t)NB * 32 * 32 * 128;
constexpr size_t WS_PROG = 4u << 20;
constexpr u32 TAG_STEP = 0x9E3779B9u;
constexpr u32 SPIN_CAP = 1u << 18;
constexpr int WAITN = 19, WMIN = WAITN - 2, WMAX = WAITN + 3;
constexpr int RUNLEN = 16;
constexpr int PRE = 5;
constexpr float CGAIN = 400.f;
constexpr int TAU = 134;
constexpr int PSHIFT = 9, PSTRIDE = 256, CSTOP = 24;
constexpr int AUX_SC0 = 1, AUX_NT = 2;

__device__ __forceinline__ float bf_lo(u32 u) { return __uint_as_float(u << 16); }
__device__ __forceinline__ float bf_hi(u32 u) { return __uint_as_float(u & 0xffff0000u); }
__device__ __forceinline__ u32 pk_bf16(float lo, float hi) {
    bf16x2 v = {(__bf16)lo, (__bf16)hi};
    return __builtin_bit_cast(u32, v);
}
__device__ __forceinline__ float bfr(float v) { return bf_lo(pk_bf16(v, v)); }
__device__ __forceinline__ float dot2(u32 a, u32 b, float acc) {
    return __builtin_amdgcn_fdot2_f32_bf16(__builtin_bit_cast(bf16x2, a), __builtin_bit_cast(bf16x2, b), acc, false);
}
template <int CTRL> __device__ __forceinline__ float dpp(float v) {
    return __builtin_bit_cast(float, __builtin_amdgcn_mov_dpp(__builtin_bit_cast(int, v), CTRL, 0xf, 0xf, true));
}
constexpr int XOR1 = 0xB1, XOR2 = 0x4E, XOR7 = 0x141, XOR8 = 0x128;
__device__ __forceinline__ float fold32(float a, float b) {
    auto s = __builtin_amdgcn_permlane32_swap(__float_as_uint(a), __float_as_uint(b), false, false);
    return __uint_as_float(s[0]) + __uint_as_float(s[1]);
}
__device__ __forceinline__ float fold16(float a, float b) {
    auto s = __builtin_amdgcn_permlane16_swap(__float_as_uint(a), __float_as_uint(b), false, false);
    return __uint_as_float(s[0]) + __uint_as_float(s[1]);
}
__device__ __forceinline__ float wave_sum(float v) {
    v += dpp<XOR1>(v); v += dpp<XOR2>(v); v += dpp<XOR7>(v); v += dpp<XOR8>(v);
    v = fold16(v, v); return fold32(v, v);
}
__device__ __forceinline__ float wave_max(float v) {
    v = fmaxf(v, dpp<XOR1>(v)); v = fmaxf(v, dpp<XOR2>(v)); v = fmaxf(v, dpp<XOR7>(v)); v = fmaxf(v, dpp<XOR8>(v));
    auto s = __builtin_amdgcn_permlane16_swap(__float_as_uint(v), __float_as_uint(v), false, false);
    v = fmaxf(__uint_as_float(s[0]), __uint_as_float(s[1]));
    auto t = __builtin_amdgcn_permlane32_swap(__float_as_uint(v), __float_as_uint(v), false, false);
    return fmaxf(__uint_as_float(t[0]), __uint_as_float(t[1]));
}
__device__ __forceinline__ v2f fold_rows(const float (&a)[8]) {
    const float x0 = fold32(a[0], a[4]), x1 = fold32(a[1], a[5]), x2 = fold32(a[2], a[6]), x3 = fold32(a[3], a[7]);
    v2f o; o.x = fold16(x0, x2); o.y = fold16(x1, x3); return o;
}
__device__ __forceinline__ void ln8(const float (&v)[8], float (&o)[8]) {
    float s = 0.f;
#pragma unroll
    for (int i = 0; i < 8; ++i) s += v[i];
    const float mean = wave_sum(s) * (1.f / DM);
    float q = 0.f;
#pragma unroll
    for (int i = 0; i < 8; ++i) { const float d = v[i] - mean; q = fmaf(d, d, q); }
    const float var = bfr(wave_sum(q) * (1.f / DM));
    const float m = bfr(mean);
    const float den = bfr(__builtin_amdgcn_sqrtf(bfr(var + 1e-5f)));
    const float rc = __builtin_amdgcn_rcpf(den);
#pragma unroll
    for (int i = 0; i < 8; ++i) {
        const float d = bfr(v[i] - m), t = d * rc;
        o[i] = fmaf(fmaf(-t, den, d), rc, t);
    }
}
__device__ __forceinline__ void unpack8(v4u u, float (&v)[8]) {
#pragma unroll
    for (int e = 0; e < 4; ++e) { v[2 * e] = bf_lo(u[e]); v[2 * e + 1] = bf_hi(u[e]); }
}
__device__ __forceinline__ void store16_agent(GAS char* p, v4u v) {
    asm volatile("global_store_dwordx4 %0, %1, off sc1\n\ts_nop 1" :: "v"(p), "v"(v) : "memory");
}
#define LGKM0() asm volatile("s_waitcnt lgkmcnt(0)" ::: "memory")
#define CFENCE() asm volatile("" ::: "memory")
#define PIN16(w, o) asm volatile("" : "+v"(w[o + 0]), "+v"(w[o + 1]), "+v"(w[o + 2]), "+v"(w[o + 3]), "+v"(w[o + 4]), "+v"(w[o + 5]), "+v"(w[o + 6]), "+v"(w[o + 7]), \
                                      "+v"(w[o + 8]), "+v"(w[o + 9]), "+v"(w[o + 10]), "+v"(w[o + 11]), "+v"(w[o + 12]), "+v"(w[o + 13]), "+v"(w[o + 14]), "+v"(w[o + 15]) :: "memory")

__device__ __forceinline__ void pair_rows(v4u (&w)[32]) {
#pragma unroll
    for (int i = 0; i < 16; ++i)
#pragma unroll
        for (int e = 0; e < 4; ++e) {
            const u32 a = w[2 * i][e], b = w[2 * i + 1][e];
            w[2 * i][e] = __builtin_amdgcn_perm(b, a, 0x05040100u);
            w[2 * i + 1][e] = __builtin_amdgcn_perm(b, a, 0x07060302u);
        }
}
__device__ __forceinline__ v2f gemv32(const v4u (&w)[32], const LAS v4u* vec  ) {
    v4u p[4];
#pragma unroll
    for (int j = 0; j < 4; ++j) p[j] = vec[j];
    float a[8];
#pragma unroll
    for (int e = 0; e < 8; ++e) a[e] = 0.f;
#pragma unroll
    for (int i = 0; i < 16; ++i)
#pragma unroll
        for (int e = 0; e < 4; ++e) {
            a[2 * e] = dot2(w[2 * i][e], p[i >> 2][i & 3], a[2 * e]);
            a[2 * e + 1] = dot2(w[2 * i + 1][e], p[i >> 2][i & 3], a[2 * e + 1]);
        }
    return fold_rows(a);
}
__device__ __forceinline__ void octet_rows(v4u (&w)[32]) {
    v4u o[32];
#pragma unroll
    for (int j = 0; j < 4; ++j)
#pragma unroll
        for (int t = 0; t < 4; ++t)
#pragma unroll
            for (int e = 0; e < 4; ++e) {
                const u32 a = w[8 * j + 2 * t][e], b = w[8 * j + 2 * t + 1][e];
                o[4 * (2 * e) + j][t] = __builtin_amdgcn_perm(b, a, 0x05040100u);
                o[4 * (2 * e + 1) + j][t] = __builtin_amdgcn_perm(b, a, 0x07060302u);
            }
#pragma unroll
    for (int i = 0; i < 32; ++i) w[i] = o[i];
}
template <int J> __device__ __forceinline__ v4u bcast4(u32 pr) {
    return (v4u){(u32)__builtin_amdgcn_mov_dpp(pr, 0x150 + 4 * J, 0xf, 0xf, true), (u32)__builtin_amdgcn_mov_dpp(pr, 0x151 + 4 * J, 0xf, 0xf, true),
                 (u32)__builtin_amdgcn_mov_dpp(pr, 0x152 + 4 * J, 0xf, 0xf, true), (u32)__builtin_amdgcn_mov_dpp(pr, 0x153 + 4 * J, 0xf, 0xf, true)};
}
__device__ __forceinline__ v2f gemv32_mfma(const v4u (&w)[32], u32 pr, bool r1, bool r2) {
    const v4u p[4] = {bcast4<0>(pr), bcast4<1>(pr), bcast4<2>(pr), bcast4<3>(pr)};
    v4f a[8];
#pragma unroll
    for (int e = 0; e < 8; ++e) a[e] = (v4f){0.f, 0.f, 0.f, 0.f};
#pragma unroll
    for (int j = 0; j < 4; ++j)
#pragma unroll
        for (int e = 0; e < 8; ++e)
            a[e] = __builtin_amdgcn_mfma_f32_16x16x32_bf16(__builtin_bit_cast(bf16x8, p[j]), __builtin_bit_cast(bf16x8, w[4 * e + j]), a[e], 0, 0, 0);
    v2f o;
    o.x = r2 ? (r1 ? a[6].x : a[4].x) : (r1 ? a[2].x : a[0].x);
    o.y = r2 ? (r1 ? a[7].x : a[5].x) : (r1 ? a[3].x : a[1].x);
    return o;
}
__device__ __forceinline__ void cbar(LAS char* L, int which, int ci, u32& budget) {
    LGKM0();
    *(volatile LAS u32*)(L + CT_FLAG + which * 16 + ci * 4) = 1u;
    for (;;) {
        const v4u f = *(volatile LAS v4u*)(L + CT_FLAG + which * 16);
        if (__builtin_amdgcn_readfirstlane(f.x & f.y & f.z & f.w)) break;
        if (budget == 0) break;
        --budget;
    }
    CFENCE();
}
__device__ __forceinline__ void wait_landed(LAS char* L, u32 tile, u32& landed, u32& budget) {
    while (landed <= tile) {
        landed = __builtin_amdgcn_readfirstlane(*(volatile LAS u32*)(L + CT_LANDED));
        if (landed > tile || budget == 0) break;
        --budget;
        __builtin_amdgcn_s_sleep(1);
    }
    CFENCE();
}

constexpr int ABIT[13] = {8, 10, 13, 16, 9, 11, 19, 15, 18, 17, 20, 12, 14};
constexpr u32 abits(u32 v, int first, int n) {
    u32 o = 0;
    for (int i = 0; i < n; ++i) o |= (v >> i & 1) << ABIT[first + i];
    return o;
}
static_assert(abits(0x1fff, 0, 13) == 0x1fff00, "a permutation of the row bits");
template <int FIRST, int N> __device__ __forceinline__ u32 abits_of(u32 v) {
    u32 o = 0;
#pragma unroll
    for (int i = 0; i < N; ++i) o |= (v >> i & 1) << ABIT[FIRST + i];
    return o;
}
constexpr u64 PRE_OFFS = (u64)(abits(1, 4, 9) >> 8) | (u64)(abits(2, 4, 9) >> 8) << 16 | (u64)(abits(3, 4, 9) >> 8) << 32 | (u64)(abits(4, 4, 9) >> 8) << 48;
static_assert(PRE <= 5 && RUNLEN == 16, "prologue offsets, run table");
constexpr int LAGE = (WMAX + 3) / 4;
static_assert(RING_OFF == 0 && NSLOT * TILE == 0x20000 && WMAX + 4 <= 63 && WMAX == WMIN + 5 && 4 * PRE <= 63, "loader loop constants");
__device__ __forceinline__ void stream_tiles(u32 win, const GAS char* src, u32 tab, u32 li, const u32 (&vo)[4], u32 t, u32 end, u32 pub_addr) {
    u32 tmp, n = end - t, pub = t - ((win + 3) >> 2);
    const u32 slot = RING_OFF + (t & (NSLOT - 1)) * TILE;
    const u32 skip = 4 * (win - WMIN);
    const u32 slo = (u32)(size_t)src, shi = (u32)((size_t)src >> 32);
    asm volatile("v_readlane_b32 s44, %[tab], %[li]\n\t"
                 "s_getpc_b64 s[40:41]\n"
                 "1:\n\t"
                 "s_add_u32 s40, s40, %[skip]\n\t"
                 "s_addc_u32 s41, s41, 0\n\t"
                 "s_add_u32 s40, s40, 2f-1b\n\t"
                 "s_addc_u32 s41, s41, 0\n\t"
                 "s_mov_b32 m0, %[slot]\n\t"
                 "s_add_u32 s42, %[slo], s44\n\t"
                 "s_addc_u32 s43, %[shi], 0\n\t"
                 "s_setpc_b64 s[40:41]\n"
                 "2:\n\t"
                 "s_waitcnt vmcnt(%c[w0])\n\t"
                 "s_waitcnt vmcnt(%c[w1])\n\t"
                 "s_waitcnt vmcnt(%c[w2])\n\t"
                 "s_waitcnt vmcnt(%c[w3])\n\t"
                 "s_waitcnt vmcnt(%c[w4])\n\t"
                 "s_waitcnt vmcnt(%c[w5])\n\t"
                 "global_load_lds_dwordx4 %[v0], s[42:43] nt\n\t"
                 "global_load_lds_dwordx4 %[v1], s[42:43] offset:1024 nt\n\t"
                 "global_load_lds_dwordx4 %[v2], s[42:43] offset:2048 nt\n\t"
                 "global_load_lds_dwordx4 %[v3], s[42:43] offset:3072 nt\n\t"
                 "v_max_i32 %[tmp], 0, %[pub]\n\t"
                 "s_add_u32 %[li], %[li], 1\n\t"
                 "ds_write_b32 %[pa], %[tmp]\n\t"
                 "v_add_u32 %[pub], 1, %[pub]\n\t"
                 "v_readlane_b32 s44, %[tab], %[li]\n\t"
                 "s_add_u32 m0, m0, 0x1000\n\t"
                 "s_and_b32 m0, m0, 0x1f000\n\t"
                 "s_add_u32 s42, %[slo], s44\n\t"
                 "s_addc_u32 s43, %[shi], 0\n\t"
                 "s_sub_u32 %[n], %[n], 1\n\t"
                 "s_cmp_eq_u32 %[n], 0\n\t"
                 "s_cbranch_scc1 3f\n\t"
                 "s_setpc_b64 s[40:41]\n"
                 "3:"
                 : [pub] "+v"(pub), [n] "+s"(n), [li] "+s"(li), [tmp] "=&v"(tmp)
                 : [slo] "s"(slo), [shi] "s"(shi), [pa] "v"(pub_addr), [slot] "s"(slot), [skip] "s"(skip), [tab] "v"(tab),
                   [v0] "v"(vo[0]), [v1] "v"(vo[1]), [v2] "v"(vo[2]), [v3] "v"(vo[3]),
                   [w0] "i"(WMIN), [w1] "i"(WMIN + 1), [w2] "i"(WMIN + 2), [w3] "i"(WMIN + 3), [w4] "i"(WMIN + 4), [w5] "i"(WMIN + 5)
                 : "scc", "s40", "s41", "s42", "s43", "s44", "memory");
}
template <int N> __device__ __forceinline__ void publish_tail(u32 pub_addr) {
    asm volatile("s_waitcnt vmcnt(%c2)\n\tds_write_b32 %0, %1" :: "v"(pub_addr), "v"(2 * TPH - N), "i"(4 * N) : "memory");
    if constexpr (N > 0) publish_tail<N - 1>(pub_addr);
}

__global__ void __launch_bounds__(384, 2)
fused_decode_kernel(const char* __restrict__ kc_, const char* __restrict__ wq_, const char* __restrict__ x_, char* ws_,
                    const char* __restrict__ vc_, const char* __restrict__ wo_,
                    const char* __restrict__ w1_, const char* __restrict__ b1_, const char* __restrict__ w2_,
                    const char* __restrict__ b2_, u16* __restrict__ out_) {
    const GAS char* x = (const GAS char*)x_; const GAS char* kc = (const GAS char*)kc_; const GAS char* vc = (const GAS char*)vc_;
    const GAS char* w1 = (const GAS char*)w1_; const GAS char* b1 = (const GAS char*)b1_; const GAS char* w2 = (const GAS char*)w2_;
    const GAS char* b2 = (const GAS char*)b2_; GAS u16* out = (GAS u16*)out_; GAS char* ws = (GAS char*)ws_;
    __shared__ __attribute__((aligned(1024))) char smem[LDS_BYTES];
    LAS char* L = (LAS char*)smem;
    const int lane = threadIdx.x & 63;
    const int wave = __builtin_amdgcn_readfirstlane(threadIdx.x >> 6);
    const int h = blockIdx.x & 31, b = blockIdx.x >> 5;
    const int wg = b * 32 + h;
    u32 budget = SPIN_CAP;
    const u64 dg_r0 = __builtin_amdgcn_s_memrealtime();

    const GAS char* src = kc + (size_t)wg * (SK * DH * 2) - 3072;
    u32 vo[4];
#pragma unroll
    for (int g = 0; g < 4; ++g)
        vo[g] = (lane & 15) * 16 + abits_of<0, 2>(lane >> 4) + abits(g, 2, 2) + (3 - g) * 1024;
    {
        u64 pk = PRE_OFFS; u32 xo = 0;
        asm volatile("s_cmp_lg_u32 %[wave], 0\n\t"
                     "s_cbranch_scc1 2f\n\t"
                     "s_mov_b32 m0, 0\n\t"
                     "s_setprio 3\n"
                     "1:\n\t"
                     "s_add_u32 s42, %[slo], %[x]\n\t"
                     "s_addc_u32 s43, %[shi], 0\n\t"
                     "s_and_b32 %[x], s44, 0xffff\n\t"
                     "global_load_lds_dwordx4 %[v0], s[42:43] nt\n\t"
                     "global_load_lds_dwordx4 %[v1], s[42:43] offset:1024 nt\n\t"
                     "global_load_lds_dwordx4 %[v2], s[42:43] offset:2048 nt\n\t"
                     "global_load_lds_dwordx4 %[v3], s[42:43] offset:3072 nt\n\t"
                     "s_add_u32 m0, m0, 0x1000\n\t"
                     "s_lshl_b32 %[x], %[x], 8\n\t"
                     "s_lshr_b64 s[44:45], s[44:45], 16\n\t"
                     "s_cmp_lg_u32 m0, %c[end]\n\t"
                     "s_cbranch_scc1 1b\n"
                     "2:"
                     : [x] "+s"(xo), "+{s[44:45]}"(pk) : [slo] "s"((u32)(size_t)src), [shi] "s"((u32)((size_t)src >> 32)), [wave] "s"(wave), [end] "i"(PRE * TILE),
                       [v0] "v"(vo[0]), [v1] "v"(vo[1]), [v2] "v"(vo[2]), [v3] "v"(vo[3]), "s"(wq_), "s"(x_), "s"(ws_) : "scc", "s42", "s43", "memory");
    }

    if (wave == 0) {
        const u32 lbase = (u32)(size_t)L;
        asm volatile("s_barrier" ::: "memory");
        const u32 pub_addr = lbase + CT_LANDED + lane * 4;
        const u32 free_addr = lbase + CT_FREE;
        const u32 tlo = abits_of<4, 4>(lane), thi = abits_of<8, 5>(lane);
        u32 t = PRE;
        for (;;) {
            v2u f0, f1; u32 wv;
            asm volatile("ds_read2st64_b32 %0, %3 offset0:0 offset1:1\n\tds_read2st64_b32 %1, %3 offset0:2 offset1:3\n\t"
                         "ds_read_b32 %2, %3 offset:%c4\n\ts_waitcnt lgkmcnt(0)"
                         : "=&v"(f0), "=&v"(f1), "=&v"(wv) : "v"(free_addr), "i"(CT_WIN - CT_FREE) : "memory");
            u32 limit = 4 * __builtin_amdgcn_readfirstlane(min(min(f0.x, f0.y), min(f1.x, f1.y))) + NSLOT;
            const u32 win = __builtin_amdgcn_readfirstlane(min(max(wv, (u32)WMIN), (u32)WMAX));
            if (t >= limit) {
                if (budget != 0) { --budget; __builtin_amdgcn_s_sleep(1); continue; }
                limit = ~0u;
            }
            const u32 end = min(limit, (t | (RUNLEN - 1)) + 1);
            stream_tiles(win, src + (u32)__builtin_amdgcn_readlane(thi, (t >> 4) & 31), tlo, t & 15, vo, t, end, pub_addr);
            t = end;
            if (t == TPH) src = vc + (size_t)wg * (SK * DH * 2) - 3072;
            if (t == 2 * TPH) break;
        }
        publish_tail<LAGE - 1>(pub_addr);
        if (lane == 0) __hip_atomic_store((GAS u32*)(ws + (5u << 20)) + blockIdx.x, (u32)__builtin_amdgcn_s_memrealtime(), RLX_AGENT);
        asm volatile("s_waitcnt lgkmcnt(0)\n\ts_barrier" ::: "memory");
    } else if (wave == 5) {
        {
            const v4u z = {0u, 0u, 0u, 0u};
            volatile LAS v4u* cw = (volatile LAS v4u*)(L + CT_OFF);
            cw[lane] = z;
            if (lane < CT_BYTES / 16 - 64) cw[64 + lane] = z;
            *(volatile LAS u32*)(L + CT_WIN) = WAITN;
        }
        LGKM0();
        asm volatile("s_barrier" ::: "memory");
        __builtin_amdgcn_sched_barrier(0);
        const v4u xr = *(const GAS v4u*)(x + b * (DM * 2) + lane * 16);
        const u32 xo = *(const GAS u16*)(x + (b * DM + 16 * h + (lane & 15)) * 2);
        const u32 b2r = *(const GAS u16*)b2;
        const u32 tag0 = __hip_atomic_load((GAS u32*)(ws + WS_EPOCH), RLX_AGENT) + TAG_STEP;
        v2u w1r[7], b1r[7], w2r[7];
#pragma unroll
        for (int k = 0; k < 7; ++k) {
            w1r[k] = *(const GAS v2u*)(w1 + lane * 56 + k * 8);
            b1r[k] = *(const GAS v2u*)(b1 + lane * 56 + k * 8);
            w2r[k] = *(const GAS v2u*)(w2 + lane * 56 + k * 8);
        }
        {
            float xv[8], xn[8], hv[8];
            unpack8(xr, xv);
            ln8(xv, xn);
#pragma unroll
            for (int i = 0; i < 8; ++i) xn[i] = bfr(xn[i]);
            ln8(xn, hv);
#pragma unroll
            for (int i = 0; i < 8; ++i) hv[i] = bfr(hv[i]);
            v4f a = {hv[0], hv[1], hv[2], hv[3]}, bb = {hv[4], hv[5], hv[6], hv[7]};
            *(LAS v4f*)(L + GH_OFF + lane * 32) = a;
            *(LAS v4f*)(L + GH_OFF + lane * 32 + 16) = bb;
        }
        float gate = 0.f;
#pragma unroll 1
        for (int d = 0; d < 16; ++d) {
            const float hd = *(const LAS float*)(L + GH_OFF + (16 * h + d) * 4);
            float y = 0.f;
#pragma unroll
            for (int k = 0; k < 14; ++k) {
                const u32 ww = w1r[k >> 1][k & 1], bb = b1r[k >> 1][k & 1], w2 = w2r[k >> 1][k & 1];
                const u32 t = pk_bf16(hd * bf_lo(ww), hd * bf_hi(ww));
                const u32 uu = pk_bf16(bf_lo(t) + bf_lo(bb), bf_hi(t) + bf_hi(bb));
                const float u0 = bf_lo(uu), u1 = bf_hi(uu);
                constexpr float C0 = -2.f * 0.7978845608028654f * 1.4426950408889634f, C1 = C0 * 0.044715f;
                const float z0 = u0 * fmaf(u0 * u0, C1, C0), z1 = u1 * fmaf(u1 * u1, C1, C0);
                const float g0 = u0 * __builtin_amdgcn_rcpf(1.f + __builtin_amdgcn_exp2f(z0));
                const float g1 = u1 * __builtin_amdgcn_rcpf(1.f + __builtin_amdgcn_exp2f(z1));
                y = dot2(pk_bf16(g0, g1), w2, y);
            }
            y = bfr(bfr(wave_sum(y)) + bf_lo(b2r));
            const float gd = bfr(y / (1.f + __expf(-y)));
            if ((lane & 15) == d) gate = gd;
        }
        {
            u32 ref = 0, have = 0, per = (u32)__builtin_amdgcn_s_memrealtime() >> PSHIFT;
            for (;;) {
                const u32 ld = __builtin_amdgcn_readfirstlane(*(volatile LAS u32*)(L + CT_LANDED));
                if (ld >= 2 * TPH - CSTOP || budget == 0) break;
                --budget;
                const u32 now = (u32)__builtin_amdgcn_s_memrealtime();
                const u32 lag = now * 10u - ld * TAU;
                if ((now >> PSHIFT) != per) {
                    per = now >> PSHIFT;
                    GAS char* pw = ws + WS_PROG + (per & 63) * (8 * PSTRIDE), *pr = ws + WS_PROG + ((per - 1) & 63) * (8 * PSTRIDE);
                    if (b == 0 && lane == 0) __hip_atomic_store((GAS u64*)(pw + (h & 7) * PSTRIDE + (h >> 3) * 8), ((u64)(tag0 + per) << 32) | lag, RLX_AGENT);
                    const u64 e = __hip_atomic_load((GAS u64*)(pr + (lane & 7) * PSTRIDE + ((lane >> 3) & 3) * 8), RLX_AGENT);
                    const bool ok = (u32)(e >> 32) == tag0 + per - 1;
                    const int n = __builtin_popcountll(__ballot(ok));
                    const float d = wave_sum(ok ? (float)(int)((u32)e - lag) : 0.f);
                    if (n >= 48) { ref = lag + __builtin_amdgcn_readfirstlane((int)(d / (float)n)); have = 1; }
                }
                if (have) {
                    const float dev = (float)(int)(lag - ref);
                    const float wf = fminf(fmaxf((float)WAITN + __builtin_rintf(dev * (1.f / CGAIN)), (float)WMIN), (float)WMAX);
                    *(volatile LAS u32*)(L + CT_WIN) = (u32)wf;
                }
                __builtin_amdgcn_s_sleep(32);
            }
        }
        asm volatile("s_barrier" ::: "memory");
        __builtin_amdgcn_s_sleep(28);
        CFENCE();
        __builtin_amdgcn_s_setprio(3);
        const u32 tag = __builtin_amdgcn_readfirstlane(*(volatile LAS u32*)(L + CT_TAG));
        GAS u64* in = (GAS u64*)(ws + WS_INBOX + (size_t)wg * 4096) + lane;
        u64 v[8];
#pragma unroll
        for (int k = 0; k < 8; ++k) v[k] = 0;
        u32 pend = 0xffu;
        for (;;) {
#pragma unroll
            for (int k = 0; k < 8; ++k)
                if (pend & (1u << k)) v[k] = __hip_atomic_load(in + k * 64, RLX_AGENT);
#pragma unroll
            for (int k = 0; k < 8; ++k)
                if (__all((u32)(v[k] >> 32) == tag)) pend &= ~(1u << k);
            if (pend == 0 || budget == 0) break;
            --budget;
        }
        float s = 0.f;
#pragma unroll
        for (int k = 0; k < 8; ++k) s += __uint_as_float((u32)v[k]);
        s = fold16(s, s);
        s = fold32(s, s);
        if (blockIdx.x == DGWG) {
            while ((u32)__builtin_amdgcn_s_memrealtime() - (u32)dg_r0 < 15800) __builtin_amdgcn_s_sleep(1);
            float e[4], sm = 0.f, mk = 0.f;
            for (int k = 0; k < 4; ++k) { e[k] = (float)(int)(__hip_atomic_load((GAS u32*)(ws + (5u << 20)) + k * 64 + lane, RLX_AGENT) - (u32)dg_r0); sm += e[k]; mk = fmaxf(mk, e[k] * 256.f + (float)(k * 64 + lane)); }
            const float mean = wave_sum(sm) * (1.f / 256);
            const int key = (int)wave_max(mk);
            const float late = (float)(key >> 8) - mean;
            const int d2 = min(max((int)(late * (1.f / 40)), 0), 15);
            const u32 ticks = __builtin_amdgcn_readfirstlane(16000 + 100 * ((key & 255) + 256 * d2));
            while ((u32)__builtin_amdgcn_s_memrealtime() - (u32)dg_r0 < ticks) __builtin_amdgcn_s_sleep(1);
        }
        const float res = bf_lo(xo) + bfr(bfr(s) * gate);
        if (lane < 16) out[b * DM + 16 * h + lane] = (u16)pk_bf16(res, res);
        if (h == 0 && lane == 0) __hip_atomic_store((GAS u32*)(ws + WS_EPOCH + b * 128), tag, RLX_AGENT);
    } else {
        __builtin_amdgcn_s_setprio(2);
        const int ci = wave - 1, r = lane >> 4, c = lane & 15;
        v4u xr = *(const GAS v4u*)(x + b * (DM * 2) + lane * 16);
        u32 tag = __hip_atomic_load((GAS u32*)(ws + WS_EPOCH + b * 128), RLX_AGENT);
        v4u w[32];
        const int rot = b;
        {
            const __amdgpu_buffer_rsrc_t rs = __builtin_amdgcn_make_buffer_rsrc((void*)wq_, 0, DM * NH * DH * 2, 0x00020000);
            const u32 vo = 32 * r * (NH * DH * 2) + c * 16, so = 128 * ci * (NH * DH * 2) + h * (DH * 2);
#pragma unroll
            for (int i = 0; i < 32; ++i)
                w[i] = __builtin_amdgcn_raw_buffer_load_b128(rs, vo, so + ((i + 4 * rot) & 31) * (NH * DH * 2), AUX_NT);
        }
        __builtin_amdgcn_sched_barrier(0);
        asm volatile("s_barrier" ::: "memory");
        asm volatile("" : "+v"(xr), "+v"(tag) :: "memory");
        tag += TAG_STEP;
        LAS char* priv = L + PV_OFF + ci * 1024;
        {
            float xv[8], xn[8];
            unpack8(xr, xv);
            ln8(xv, xn);
            v4u o;
#pragma unroll
            for (int e = 0; e < 4; ++e) o[e] = pk_bf16(xn[2 * e], xn[2 * e + 1]);
            v2u o0 = {o.x, o.y}, o1 = {o.z, o.w};
            *(LAS v2u*)(priv + (lane >> 2) * 64 + ((4 * (lane & 3) - 2 * rot) & 15) * 4) = o0;
            *(LAS v2u*)(priv + (lane >> 2) * 64 + ((4 * (lane & 3) + 2 - 2 * rot) & 15) * 4) = o1;
        }
        if (ci == 0) *(volatile LAS u32*)(L + CT_TAG) = tag;
        pair_rows(w);
        {
            const v2f qp = gemv32(w, (const LAS v4u*)(priv + (128 * ci + 32 * r) * 2));
            *(LAS v2f*)(L + QP_OFF + ci * 512 + (8 * c + 2 * r) * 4) = qp;
        }
        CFENCE();
        cbar(L, 0, ci, budget);
        u32 q[4];
        {
            v4f s0 = *(const LAS v4f*)(L + QP_OFF + c * 32), s1 = *(const LAS v4f*)(L + QP_OFF + c * 32 + 16);
#pragma unroll
            for (int k = 1; k < 4; ++k) { s0 += *(const LAS v4f*)(L + QP_OFF + k * 512 + c * 32); s1 += *(const LAS v4f*)(L + QP_OFF + k * 512 + c * 32 + 16); }
            q[0] = pk_bf16(s0.x, s0.y); q[1] = pk_bf16(s0.z, s0.w); q[2] = pk_bf16(s1.x, s1.y); q[3] = pk_bf16(s1.z, s1.w);
        }
        tag = *(volatile LAS u32*)(L + CT_TAG);
        volatile LAS u32* freew = (volatile LAS u32*)(L + CT_FREE + ci * 256 + lane * 4);
        LAS char* myscores = L + SC_OFF + ci * 4096;
        u32 landed = 0;
        float mx = -INFINITY;
        const bool lo8 = c < 8, lo4 = (c & 4) == 0;
        const __amdgpu_buffer_rsrc_t wors = __builtin_amdgcn_make_buffer_rsrc((void*)wo_, 0, DM * NH * DH * 2, 0x00020000);
        const u32 wovo = (h * DH + 32 * r) * (DM * 2) + (128 * ci + 8 * c) * 2;
#pragma unroll
        for (int seg = 0; seg < 8; ++seg) {
#pragma unroll
            for (int i = 4 * seg; i < 4 * seg + 4; ++i) {
                const u32 vo = wovo + (i & 3) * (DM * 2), so = ((seg + rot) & 7) * (4 * DM * 2);
                if (seg) w[i] = __builtin_amdgcn_raw_buffer_load_b128(wors, vo, so, 0);
                else w[i] = __builtin_amdgcn_raw_buffer_load_b128(wors, vo, so, AUX_NT);
            }
            if (seg == 0) {
                __builtin_amdgcn_sched_barrier(0);
#pragma unroll
                for (int i = 0; i < 4; ++i) w[28 + i] = __builtin_amdgcn_raw_buffer_load_b128(wors, wovo + i * (DM * 2), rot * (4 * DM * 2), AUX_SC0);
            }
            if (seg == 1) asm volatile("" :: "v"(w[28]), "v"(w[29]), "v"(w[30]), "v"(w[31]));
            __builtin_amdgcn_sched_barrier(0);
#pragma unroll 1
            for (int it = seg * (ITERS / 8); it < (seg + 1) * (ITERS / 8); ++it) {
                const u32 tile = 4 * it + ci;
                wait_landed(L, tile, landed, budget);
                const LAS v4u* tp = (const LAS v4u*)(L + RING_OFF + (tile & (NSLOT - 1)) * TILE + lane * 16);
                v4u k0 = tp[0], k1 = tp[64], k2 = tp[128], k3 = tp[192];
                LGKM0();
                *freew = it + 1;
                asm volatile("" : "+v"(k0), "+v"(k1), "+v"(k2), "+v"(k3) :: "memory");
                float a0 = 0.f, a1 = 0.f, a2 = 0.f, a3 = 0.f;
#pragma unroll
                for (int e = 0; e < 4; ++e) { a0 = dot2(k0[e], q[e], a0); a1 = dot2(k1[e], q[e], a1); a2 = dot2(k2[e], q[e], a2); a3 = dot2(k3[e], q[e], a3); }
                const float b0 = (lo8 ? a0 : a2) + dpp<XOR8>(lo8 ? a2 : a0);
                const float b1 = (lo8 ? a1 : a3) + dpp<XOR8>(lo8 ? a3 : a1);
                float s = (lo4 ? b0 : b1) + dpp<XOR7>(lo4 ? b1 : b0);
                s += dpp<XOR2>(s);
                s += dpp<XOR1>(s);
                const float sc = bfr(s) * 0.088388347648318440f;
                const u32 sb = pk_bf16(sc, sc);
                mx = fmaxf(mx, bf_lo(sb));
                *(LAS u16*)(myscores + it * 32 + r * 8 + (c >> 2) * 2) = (u16)sb;
            }
        }
        octet_rows(w);
        PIN16(w, 0); PIN16(w, 16);
        mx = wave_max(mx);
        *(volatile LAS float*)(L + CT_MAXV + ci * 4) = mx;
        cbar(L, 1, ci, budget);
        {
            const v4f mv = *(volatile LAS v4f*)(L + CT_MAXV);
            const float gm = fmaxf(fmaxf(mv.x, mv.y), fmaxf(mv.z, mv.w));
            LAS v4u* sp = (LAS v4u*)(myscores + lane * 16);
            float ex[32];
            float sum = 0.f;
#pragma unroll
            for (int j = 0; j < 4; ++j) {
                const v4u u = sp[j * 64];
#pragma unroll
                for (int e = 0; e < 4; ++e) {
                    const float e0 = __builtin_amdgcn_exp2f((bf_lo(u[e]) - gm) * 1.4426950408889634f);
                    const float e1 = __builtin_amdgcn_exp2f((bf_hi(u[e]) - gm) * 1.4426950408889634f);
                    ex[8 * j + 2 * e] = e0; ex[8 * j + 2 * e + 1] = e1;
                    sum += e0 + e1;
                }
            }
            sum = wave_sum(sum);
            *(volatile LAS float*)(L + CT_SUMV + ci * 4) = sum;
            cbar(L, 2, ci, budget);
            const v4f sv = *(volatile LAS v4f*)(L + CT_SUMV);
            const float inv = 1.f / ((sv.x + sv.y) + (sv.z + sv.w));
#pragma unroll
            for (int j = 0; j < 4; ++j) {
                v4u u;
#pragma unroll
                for (int e = 0; e < 4; ++e) u[e] = pk_bf16(ex[8 * j + 2 * e] * inv, ex[8 * j + 2 * e + 1] * inv);
                sp[j * 64] = u;
            }
        }
        CFENCE();
        float acc[8];
#pragma unroll
        for (int e = 0; e < 8; ++e) acc[e] = 0.f;
        u32 opw = OP_OFF + ci * 512 + (8 * c + 2 * r) * 4, opr = OP_OFF + (16 * r + ((c + 2 * rot) & 15)) * 8;
        GAS char* g = ws + WS_INBOX + ((size_t)((b * 32 + 8 * ci + (c >> 1)) * 32 + (h & 7) * 4 + (h >> 3))) * 128 + (8 * (c & 1) + 2 * r) * 8;
        asm volatile("" : "+v"(opw), "+v"(opr), "+v"(g));
#pragma unroll 1
        for (int it = 0; it < ITERS; ++it) {
            const u32 tile = TPH + 4 * it + ci;
            const v2u pp = *(const LAS v2u*)(myscores + it * 32 + r * 8);
            wait_landed(L, tile, landed, budget);
            const LAS v4u* tp = (const LAS v4u*)(L + RING_OFF + (tile & (NSLOT - 1)) * TILE + lane * 16);
            v4u v0 = tp[0], v1 = tp[64], v2 = tp[128], v3 = tp[192];
            LGKM0();
            *freew = ITERS + it + 1;
            asm volatile("" : "+v"(v0), "+v"(v1), "+v"(v2), "+v"(v3) :: "memory");
#pragma unroll
            for (int e = 0; e < 4; ++e) {
                acc[2 * e] = dot2(__builtin_amdgcn_perm(v1[e], v0[e], 0x05040100u), pp.x, acc[2 * e]);
                acc[2 * e + 1] = dot2(__builtin_amdgcn_perm(v1[e], v0[e], 0x07060302u), pp.x, acc[2 * e + 1]);
            }
#pragma unroll
            for (int e = 0; e < 4; ++e) {
                acc[2 * e] = dot2(__builtin_amdgcn_perm(v3[e], v2[e], 0x05040100u), pp.y, acc[2 * e]);
                acc[2 * e + 1] = dot2(__builtin_amdgcn_perm(v3[e], v2[e], 0x07060302u), pp.y, acc[2 * e + 1]);
            }
        }
        *(LAS v2f*)(size_t)opw = fold_rows(acc);
        asm volatile("s_waitcnt lgkmcnt(0)\n\ts_barrier" ::: "memory");
        const LAS char* op = (const LAS char*)(size_t)opr;
        const v2f o0 = *(const LAS v2f*)op, o1 = *(const LAS v2f*)(op + 512), o2 = *(const LAS v2f*)(op + 1024), o3 = *(const LAS v2f*)(op + 1536);
        const v2f o = (o0 + o1) + (o2 + o3);
        const v2f sp = gemv32_mfma(w, pk_bf16(o.x, o.y), r & 1, r & 2);
        store16_agent(g, (v4u){__float_as_uint(sp.x), tag, __float_as_uint(sp.y), tag});
    }
}

extern "C" void kernel_launch(void* const* d_in, const int* in_sizes, int n_in,
                              void* d_out, int out_size, void* d_ws, size_t ws_size,
                              hipStream_t stream) {
    (void)in_sizes; (void)n_in; (void)out_size; (void)ws_size;
    fused_decode_kernel<<<NB * NH, 384, 0, stream>>>(
        (const char*)d_in[1], (const char*)d_in[3], (const char*)d_in[0], (char*)d_ws, (const char*)d_in[2], (const char*)d_in[4],
        (const char*)d_in[6], (const char*)d_in[7], (const char*)d_in[8], (const char*)d_in[9], (u16*)d_out);
}
```

```cpp
#include <hip/hip_runtime.h>
#include <hip/hip_bf16.h>
#include <stdint.h>

#define GAS __attribute__((address_space(1)))
#define LAS __attribute__((address_space(3)))
typedef unsigned u32;
typedef unsigned short u16;
typedef unsigned long long u64;
typedef u32 v4u __attribute__((ext_vector_type(4)));
typedef u32 v2u __attribute__((ext_vector_type(2)));
typedef float v4f __attribute__((ext_vector_type(4)));
typedef float v2f __attribute__((ext_vector_type(2)));
typedef __bf16 bf16x2 __attribute__((ext_vector_type(2)));
typedef __bf16 bf16x8 __attribute__((ext_vector_type(8)));

#define RLX_AGENT __ATOMIC_RELAXED, __HIP_MEMORY_SCOPE_AGENT

constexpr int NB = 8, NH = 32, SK = 8192, DH = 128, DM = 512;
constexpr int TILE = 4096, NSLOT = 32, TPH = SK * DH * 2 / TILE;
constexpr int ITERS = TPH / 4;
constexpr int RING_OFF = 0;
constexpr int SC_OFF = RING_OFF + NSLOT * TILE;
constexpr int QP_OFF = SC_OFF + SK * 2;
constexpr int OP_OFF = QP_OFF + 2048;
constexpr int PV_OFF = OP_OFF + 2048;
constexpr int GH_OFF = PV_OFF + 4096;
constexpr int CT_OFF = GH_OFF + 2048;
constexpr int CT_LANDED = CT_OFF;
constexpr int CT_FREE = CT_OFF + 256;
constexpr int CT_FLAG = CT_OFF + 1280;
constexpr int CT_MAXV = CT_FLAG + 64, CT_SUMV = CT_FLAG + 80, CT_TAG = CT_FLAG + 96, CT_WIN = CT_FLAG + 112;
constexpr int CT_BYTES = 1536;
constexpr int LDS_BYTES = CT_OFF + CT_BYTES;
static_assert(LDS_BYTES <= 160 * 1024 && LDS_BYTES > 80 * 1024, "one workgroup per CU");
constexpr size_t WS_INBOX = 0;
constexpr size_t WS_EPOCH = (size_t)NB * 32 * 32 * 128;
constexpr size_t WS_PROG = 4u << 20;
constexpr u32 TAG_STEP = 0x9E3779B9u;
constexpr u32 SPIN_CAP = 1u << 18;
constexpr int WAITN = 19, WMIN = WAITN - 2, WMAX = WAITN + 3;
constexpr int RUNLEN = 16;
constexpr int PRE = 5;
constexpr float CGAIN = 400.f;
constexpr int TAU = 134;
constexpr int PSHIFT = 9, PSTRIDE = 256, CSTOP = 24;
constexpr int AUX_SC0 = 1, AUX_NT = 2;

__device__ __forceinline__ float bf_lo(u32 u) { return __uint_as_float(u << 16); }
__device__ __forceinline__ float bf_hi(u32 u) { return __uint_as_float(u & 0xffff0000u); }
__device__ __forceinline__ u32 pk_bf16(float lo, float hi) {
    bf16x2 v = {(__bf16)lo, (__bf16)hi};
    return __builtin_bit_cast(u32, v);
}
__device__ __forceinline__ float bfr(float v) { return bf_lo(pk_bf16(v, v)); }
__device__ __forceinline__ float dot2(u32 a, u32 b, float acc) {
    return __builtin_amdgcn_fdot2_f32_bf16(__builtin_bit_cast(bf16x2, a), __builtin_bit_cast(bf16x2, b), acc, false);
}
template <int CTRL> __device__ __forceinline__ float dpp(float v) {
    return __builtin_bit_cast(float, __builtin_amdgcn_mov_dpp(__builtin_bit_cast(int, v), CTRL, 0xf, 0xf, true));
}
constexpr int XOR1 = 0xB1, XOR2 = 0x4E, XOR7 = 0x141, XOR8 = 0x128;
__device__ __forceinline__ float fold32(float a, float b) {
    auto s = __builtin_amdgcn_permlane32_swap(__float_as_uint(a), __float_as_uint(b), false, false);
    return __uint_as_float(s[0]) + __uint_as_float(s[1]);
}
__device__ __forceinline__ float fold16(float a, float b) {
    auto s = __builtin_amdgcn_permlane16_swap(__float_as_uint(a), __float_as_uint(b), false, false);
    return __uint_as_float(s[0]) + __uint_as_float(s[1]);
}
__device__ __forceinline__ float wave_sum(float v) {
    v += dpp<XOR1>(v); v += dpp<XOR2>(v); v += dpp<XOR7>(v); v += dpp<XOR8>(v);
    v = fold16(v, v); return fold32(v, v);
}
__device__ __forceinline__ float wave_max(float v) {
    v = fmaxf(v, dpp<XOR1>(v)); v = fmaxf(v, dpp<XOR2>(v)); v = fmaxf(v, dpp<XOR7>(v)); v = fmaxf(v, dpp<XOR8>(v));
    auto s = __builtin_amdgcn_permlane16_swap(__float_as_uint(v), __float_as_uint(v), false, false);
    v = fmaxf(__uint_as_float(s[0]), __uint_as_float(s[1]));
    auto t = __builtin_amdgcn_permlane32_swap(__float_as_uint(v), __float_as_uint(v), false, false);
    return fmaxf(__uint_as_float(t[0]), __uint_as_float(t[1]));
}
__device__ __forceinline__ v2f fold_rows(const float (&a)[8]) {
    const float x0 = fold32(a[0], a[4]), x1 = fold32(a[1], a[5]), x2 = fold32(a[2], a[6]), x3 = fold32(a[3], a[7]);
    v2f o; o.x = fold16(x0, x2); o.y = fold16(x1, x3); return o;
}
__device__ __forceinline__ void ln8(const float (&v)[8], float (&o)[8]) {
    float s = 0.f;
#pragma unroll
    for (int i = 0; i < 8; ++i) s += v[i];
    const float mean = wave_sum(s) * (1.f / DM);
    float q = 0.f;
#pragma unroll
    for (int i = 0; i < 8; ++i) { const float d = v[i] - mean; q = fmaf(d, d, q); }
    const float var = bfr(wave_sum(q) * (1.f / DM));
    const float m = bfr(mean);
    const float den = bfr(__builtin_amdgcn_sqrtf(bfr(var + 1e-5f)));
    const float rc = __builtin_amdgcn_rcpf(den);
#pragma unroll
    for (int i = 0; i < 8; ++i) {
        const float d = bfr(v[i] - m), t = d * rc;
        o[i] = fmaf(fmaf(-t, den, d), rc, t);
    }
}
__device__ __forceinline__ void unpack8(v4u u, float (&v)[8]) {
#pragma unroll
    for (int e = 0; e < 4; ++e) { v[2 * e] = bf_lo(u[e]); v[2 * e + 1] = bf_hi(u[e]); }
}
__device__ __forceinline__ void store16_agent(GAS char* p, v4u v) {
    asm volatile("global_store_dwordx4 %0, %1, off sc1\n\ts_nop 1" :: "v"(p), "v"(v) : "memory");
}
#define LGKM0() asm volatile("s_waitcnt lgkmcnt(0)" ::: "memory")
#define CFENCE() asm volatile("" ::: "memory")
#define PIN16(w, o) asm volatile("" : "+v"(w[o + 0]), "+v"(w[o + 1]), "+v"(w[o + 2]), "+v"(w[o + 3]), "+v"(w[o + 4]), "+v"(w[o + 5]), "+v"(w[o + 6]), "+v"(w[o + 7]), \
                                      "+v"(w[o + 8]), "+v"(w[o + 9]), "+v"(w[o + 10]), "+v"(w[o + 11]), "+v"(w[o + 12]), "+v"(w[o + 13]), "+v"(w[o + 14]), "+v"(w[o + 15]) :: "memory")

__device__ __forceinline__ void pair_rows(v4u (&w)[32]) {
#pragma unroll
    for (int i = 0; i < 16; ++i)
#pragma unroll
        for (int e = 0; e < 4; ++e) {
            const u32 a = w[2 * i][e], b = w[2 * i + 1][e];
            w[2 * i][e] = __builtin_amdgcn_perm(b, a, 0x05040100u);
            w[2 * i + 1][e] = __builtin_amdgcn_perm(b, a, 0x07060302u);
        }
}
__device__ __forceinline__ v2f gemv32(const v4u (&w)[32], const LAS v4u* vec  ) {
    v4u p[4];
#pragma unroll
    for (int j = 0; j < 4; ++j) p[j] = vec[j];
    float a[8];
#pragma unroll
    for (int e = 0; e < 8; ++e) a[e] = 0.f;
#pragma unroll
    for (int i = 0; i < 16; ++i)
#pragma unroll
        for (int e = 0; e < 4; ++e) {
            a[2 * e] = dot2(w[2 * i][e], p[i >> 2][i & 3], a[2 * e]);
            a[2 * e + 1] = dot2(w[2 * i + 1][e], p[i >> 2][i & 3], a[2 * e + 1]);
        }
    return fold_rows(a);
}
__device__ __forceinline__ void octet_rows(v4u (&w)[32]) {
    v4u o[32];
#pragma unroll
    for (int j = 0; j < 4; ++j)
#pragma unroll
        for (int t = 0; t < 4; ++t)
#pragma unroll
            for (int e = 0; e < 4; ++e) {
                const u32 a = w[8 * j + 2 * t][e], b = w[8 * j + 2 * t + 1][e];
                o[4 * (2 * e) + j][t] = __builtin_amdgcn_perm(b, a, 0x05040100u);
                o[4 * (2 * e + 1) + j][t] = __builtin_amdgcn_perm(b, a, 0x07060302u);
            }
#pragma unroll
    for (int i = 0; i < 32; ++i) w[i] = o[i];
}
template <int J> __device__ __forceinline__ v4u bcast4(u32 pr) {
    return (v4u){(u32)__builtin_amdgcn_mov_dpp(pr, 0x150 + 4 * J, 0xf, 0xf, true), (u32)__builtin_amdgcn_mov_dpp(pr, 0x151 + 4 * J, 0xf, 0xf, true),
                 (u32)__builtin_amdgcn_mov_dpp(pr, 0x152 + 4 * J, 0xf, 0xf, true), (u32)__builtin_amdgcn_mov_dpp(pr, 0x153 + 4 * J, 0xf, 0xf, true)};
}
__device__ __forceinline__ v2f gemv32_mfma(const v4u (&w)[32], u32 pr, bool r1, bool r2) {
    const v4u p[4] = {bcast4<0>(pr), bcast4<1>(pr), bcast4<2>(pr), bcast4<3>(pr)};
    v4f a[8];
#pragma unroll
    for (int e = 0; e < 8; ++e) a[e] = (v4f){0.f, 0.f, 0.f, 0.f};
#pragma unroll
    for (int j = 0; j < 4; ++j)
#pragma unroll
        for (int e = 0; e < 8; ++e)
            a[e] = __builtin_amdgcn_mfma_f32_16x16x32_bf16(__builtin_bit_cast(bf16x8, p[j]), __builtin_bit_cast(bf16x8, w[4 * e + j]), a[e], 0, 0, 0);
    v2f o;
    o.x = r2 ? (r1 ? a[6].x : a[4].x) : (r1 ? a[2].x : a[0].x);
    o.y = r2 ? (r1 ? a[7].x : a[5].x) : (r1 ? a[3].x : a[1].x);
    return o;
}
__device__ __forceinline__ void cbar(LAS char* L, int which, int ci, u32& budget) {
    LGKM0();
    *(volatile LAS u32*)(L + CT_FLAG + which * 16 + ci * 4) = 1u;
    for (;;) {
        const v4u f = *(volatile LAS v4u*)(L + CT_FLAG + which * 16);
        if (__builtin_amdgcn_readfirstlane(f.x & f.y & f.z & f.w)) break;
        if (budget == 0) break;
        --budget;
    }
    CFENCE();
}
__device__ __forceinline__ void wait_landed(LAS char* L, u32 tile, u32& landed, u32& budget) {
    while (landed <= tile) {
        landed = __builtin_amdgcn_readfirstlane(*(volatile LAS u32*)(L + CT_LANDED));
        if (landed > tile || budget == 0) break;
        --budget;
        __builtin_amdgcn_s_sleep(1);
    }
    CFENCE();
}

constexpr int ABIT[13] = {8, 10, 13, 16, 9, 11, 19, 15, 18, 17, 20, 12, 14};
constexpr u32 abits(u32 v, int first, int n) {
    u32 o = 0;
    for (int i = 0; i < n; ++i) o |= (v >> i & 1) << ABIT[first + i];
    return o;
}
static_assert(abits(0x1fff, 0, 13) == 0x1fff00, "a permutation of the row bits");
template <int FIRST, int N> __device__ __forceinline__ u32 abits_of(u32 v) {
    u32 o = 0;
#pragma unroll
    for (int i = 0; i < N; ++i) o |= (v >> i & 1) << ABIT[FIRST + i];
    return o;
}
constexpr u64 PRE_OFFS = (u64)(abits(1, 4, 9) >> 8) | (u64)(abits(2, 4, 9) >> 8) << 16 | (u64)(abits(3, 4, 9) >> 8) << 32 | (u64)(abits(4, 4, 9) >> 8) << 48;
static_assert(PRE <= 5 && RUNLEN == 16, "prologue offsets, run table");
constexpr int LAGE = (WMAX + 3) / 4;
static_assert(RING_OFF == 0 && NSLOT * TILE == 0x20000 && WMAX + 4 <= 63 && WMAX == WMIN + 5 && 4 * PRE <= 63, "loader loop constants");
__device__ __forceinline__ void stream_tiles(u32 win, const GAS char* src, u32 tab, u32 li, const u32 (&vo)[4], u32 t, u32 end, u32 pub_addr) {
    u32 tmp, n = end - t, pub = t - ((win + 3) >> 2);
    const u32 slot = RING_OFF + (t & (NSLOT - 1)) * TILE;
    const u32 skip = 4 * (win - WMIN);
    const u32 slo = (u32)(size_t)src, shi = (u32)((size_t)src >> 32);
    asm volatile("v_readlane_b32 s44, %[tab], %[li]\n\t"
                 "s_getpc_b64 s[40:41]\n"
                 "1:\n\t"
                 "s_add_u32 s40, s40, %[skip]\n\t"
                 "s_addc_u32 s41, s41, 0\n\t"
                 "s_add_u32 s40, s40, 2f-1b\n\t"
                 "s_addc_u32 s41, s41, 0\n\t"
                 "s_mov_b32 m0, %[slot]\n\t"
                 "s_add_u32 s42, %[slo], s44\n\t"
                 "s_addc_u32 s43, %[shi], 0\n\t"
                 "s_setpc_b64 s[40:41]\n"
                 "2:\n\t"
                 "s_waitcnt vmcnt(%c[w0])\n\t"
                 "s_waitcnt vmcnt(%c[w1])\n\t"
                 "s_waitcnt vmcnt(%c[w2])\n\t"
                 "s_waitcnt vmcnt(%c[w3])\n\t"
                 "s_waitcnt vmcnt(%c[w4])\n\t"
                 "s_waitcnt vmcnt(%c[w5])\n\t"
                 "global_load_lds_dwordx4 %[v0], s[42:43] nt\n\t"
                 "global_load_lds_dwordx4 %[v1], s[42:43] offset:1024 nt\n\t"
                 "global_load_lds_dwordx4 %[v2], s[42:43] offset:2048 nt\n\t"
                 "global_load_lds_dwordx4 %[v3], s[42:43] offset:3072 nt\n\t"
                 "v_max_i32 %[tmp], 0, %[pub]\n\t"
                 "s_add_u32 %[li], %[li], 1\n\t"
                 "ds_write_b32 %[pa], %[tmp]\n\t"
                 "v_add_u32 %[pub], 1, %[pub]\n\t"
                 "v_readlane_b32 s44, %[tab], %[li]\n\t"
                 "s_add_u32 m0, m0, 0x1000\n\t"
                 "s_and_b32 m0, m0, 0x1f000\n\t"
                 "s_add_u32 s42, %[slo], s44\n\t"
                 "s_addc_u32 s43, %[shi], 0\n\t"
                 "s_sub_u32 %[n], %[n], 1\n\t"
                 "s_cmp_eq_u32 %[n], 0\n\t"
                 "s_cbranch_scc1 3f\n\t"
                 "s_setpc_b64 s[40:41]\n"
                 "3:"
                 : [pub] "+v"(pub), [n] "+s"(n), [li] "+s"(li), [tmp] "=&v"(tmp)
                 : [slo] "s"(slo), [shi] "s"(shi), [pa] "v"(pub_addr), [slot] "s"(slot), [skip] "s"(skip), [tab] "v"(tab),
                   [v0] "v"(vo[0]), [v1] "v"(vo[1]), [v2] "v"(vo[2]), [v3] "v"(vo[3]),
                   [w0] "i"(WMIN), [w1] "i"(WMIN + 1), [w2] "i"(WMIN + 2), [w3] "i"(WMIN + 3), [w4] "i"(WMIN + 4), [w5] "i"(WMIN + 5)
                 : "scc", "s40", "s41", "s42", "s43", "s44", "memory");
}
template <int N> __device__ __forceinline__ void publish_tail(u32 pub_addr) {
    asm volatile("s_waitcnt vmcnt(%c2)\n\tds_write_b32 %0, %1" :: "v"(pub_addr), "v"(2 * TPH - N), "i"(4 * N) : "memory");
    if constexpr (N > 0) publish_tail<N - 1>(pub_addr);
}

__global__ void __launch_bounds__(384, 2)
fused_decode_kernel(const char* __restrict__ kc_, const char* __restrict__ wq_, const char* __restrict__ x_, char* ws_,
                    const char* __restrict__ vc_, const char* __restrict__ wo_,
                    const char* __restrict__ w1_, const char* __restrict__ b1_, const char* __restrict__ w2_,
                    const char* __restrict__ b2_, u16* __restrict__ out_) {
    const GAS char* x = (const GAS char*)x_; const GAS char* kc = (const GAS char*)kc_; const GAS char* vc = (const GAS char*)vc_;
    const GAS char* w1 = (const GAS char*)w1_; const GAS char* b1 = (const GAS char*)b1_; const GAS char* w2 = (const GAS char*)w2_;
    const GAS char* b2 = (const GAS char*)b2_; GAS u16* out = (GAS u16*)out_; GAS char* ws = (GAS char*)ws_;
    __shared__ __attribute__((aligned(1024))) char smem[LDS_BYTES];
    LAS char* L = (LAS char*)smem;
    const int lane = threadIdx.x & 63;
    const int wave = __builtin_amdgcn_readfirstlane(threadIdx.x >> 6);
    const int h = blockIdx.x & 31, b = blockIdx.x >> 5;
    const int wg = b * 32 + h;
    u32 budget = SPIN_CAP;

    const GAS char* src = kc + (size_t)wg * (SK * DH * 2) - 3072;
    u32 vo[4];
#pragma unroll
    for (int g = 0; g < 4; ++g)
        vo[g] = (lane & 15) * 16 + abits_of<0, 2>(lane >> 4) + abits(g, 2, 2) + (3 - g) * 1024;
    {
        u64 pk = PRE_OFFS; u32 xo = 0;
        asm volatile("s_cmp_lg_u32 %[wave], 0\n\t"
                     "s_cbranch_scc1 2f\n\t"
                     "s_mov_b32 m0, 0\n\t"
                     "s_setprio 3\n"
                     "1:\n\t"
                     "s_add_u32 s42, %[slo], %[x]\n\t"
                     "s_addc_u32 s43, %[shi], 0\n\t"
                     "s_and_b32 %[x], s44, 0xffff\n\t"
                     "global_load_lds_dwordx4 %[v0], s[42:43] nt\n\t"
                     "global_load_lds_dwordx4 %[v1], s[42:43] offset:1024 nt\n\t"
                     "global_load_lds_dwordx4 %[v2], s[42:43] offset:2048 nt\n\t"
                     "global_load_lds_dwordx4 %[v3], s[42:43] offset:3072 nt\n\t"
                     "s_add_u32 m0, m0, 0x1000\n\t"
                     "s_lshl_b32 %[x], %[x], 8\n\t"
                     "s_lshr_b64 s[44:45], s[44:45], 16\n\t"
                     "s_cmp_lg_u32 m0, %c[end]\n\t"
                     "s_cbranch_scc1 1b\n"
                     "2:"
                     : [x] "+s"(xo), "+{s[44:45]}"(pk) : [slo] "s"((u32)(size_t)src), [shi] "s"((u32)((size_t)src >> 32)), [wave] "s"(wave), [end] "i"(PRE * TILE),
                       [v0] "v"(vo[0]), [v1] "v"(vo[1]), [v2] "v"(vo[2]), [v3] "v"(vo[3]), "s"(wq_), "s"(x_), "s"(ws_) : "scc", "s42", "s43", "memory");
    }

    if (wave == 0) {
        const u32 lbase = (u32)(size_t)L;
        asm volatile("s_barrier" ::: "memory");
        const u32 pub_addr = lbase + CT_LANDED + lane * 4;
        const u32 free_addr = lbase + CT_FREE;
        const u32 tlo = abits_of<4, 4>(lane), thi = abits_of<8, 5>(lane);
        u32 t = PRE;
        for (;;) {
            v2u f0, f1; u32 wv;
            asm volatile("ds_read2st64_b32 %0, %3 offset0:0 offset1:1\n\tds_read2st64_b32 %1, %3 offset0:2 offset1:3\n\t"
                         "ds_read_b32 %2, %3 offset:%c4\n\ts_waitcnt lgkmcnt(0)"
                         : "=&v"(f0), "=&v"(f1), "=&v"(wv) : "v"(free_addr), "i"(CT_WIN - CT_FREE) : "memory");
            u32 limit = 4 * __builtin_amdgcn_readfirstlane(min(min(f0.x, f0.y), min(f1.x, f1.y))) + NSLOT;
            const u32 win = __builtin_amdgcn_readfirstlane(min(max(wv, (u32)WMIN), (u32)WMAX));
            if (t >= limit) {
                if (budget != 0) { --budget; __builtin_amdgcn_s_sleep(1); continue; }
                limit = ~0u;
            }
            const u32 end = min(limit, (t | (RUNLEN - 1)) + 1);
            stream_tiles(win, src + (u32)__builtin_amdgcn_readlane(thi, (t >> 4) & 31), tlo, t & 15, vo, t, end, pub_addr);
            t = end;
            if (t == TPH) src = vc + (size_t)wg * (SK * DH * 2) - 3072;
            if (t == 2 * TPH) break;
        }
        publish_tail<LAGE - 1>(pub_addr);
        asm volatile("s_waitcnt lgkmcnt(0)\n\ts_barrier" ::: "memory");
    } else if (wave == 5) {
        {
            const v4u z = {0u, 0u, 0u, 0u};
            volatile LAS v4u* cw = (volatile LAS v4u*)(L + CT_OFF);
            cw[lane] = z;
            if (lane < CT_BYTES / 16 - 64) cw[64 + lane] = z;
            *(volatile LAS u32*)(L + CT_WIN) = WAITN;
        }
        LGKM0();
        asm volatile("s_barrier" ::: "memory");
        __builtin_amdgcn_sched_barrier(0);
        const v4u xr = *(const GAS v4u*)(x + b * (DM * 2) + lane * 16);
        const u32 xo = *(const GAS u16*)(x + (b * DM + 16 * h + (lane & 15)) * 2);
        const u32 b2r = *(const GAS u16*)b2;
        const u32 tag0 = __hip_atomic_load((GAS u32*)(ws + WS_EPOCH), RLX_AGENT) + TAG_STEP;
        v2u w1r[7], b1r[7], w2r[7];
#pragma unroll
        for (int k = 0; k < 7; ++k) {
            w1r[k] = *(const GAS v2u*)(w1 + lane * 56 + k * 8);
            b1r[k] = *(const GAS v2u*)(b1 + lane * 56 + k * 8);
            w2r[k] = *(const GAS v2u*)(w2 + lane * 56 + k * 8);
        }
        {
            float xv[8], xn[8], hv[8];
            unpack8(xr, xv);
            ln8(xv, xn);
#pragma unroll
            for (int i = 0; i < 8; ++i) xn[i] = bfr(xn[i]);
            ln8(xn, hv);
#pragma unroll
            for (int i = 0; i < 8; ++i) hv[i] = bfr(hv[i]);
            v4f a = {hv[0], hv[1], hv[2], hv[3]}, bb = {hv[4], hv[5], hv[6], hv[7]};
            *(LAS v4f*)(L + GH_OFF + lane * 32) = a;
            *(LAS v4f*)(L + GH_OFF + lane * 32 + 16) = bb;
        }
        float gate = 0.f;
#pragma unroll 1
        for (int d = 0; d < 16; ++d) {
            const float hd = *(const LAS float*)(L + GH_OFF + (16 * h + d) * 4);
            float y = 0.f;
#pragma unroll
            for (int k = 0; k < 14; ++k) {
                const u32 ww = w1r[k >> 1][k & 1], bb = b1r[k >> 1][k & 1], w2 = w2r[k >> 1][k & 1];
                const u32 t = pk_bf16(hd * bf_lo(ww), hd * bf_hi(ww));
                const u32 uu = pk_bf16(bf_lo(t) + bf_lo(bb), bf_hi(t) + bf_hi(bb));
                const float u0 = bf_lo(uu), u1 = bf_hi(uu);
                constexpr float C0 = -2.f * 0.7978845608028654f * 1.4426950408889634f, C1 = C0 * 0.044715f;
                const float z0 = u0 * fmaf(u0 * u0, C1, C0), z1 = u1 * fmaf(u1 * u1, C1, C0);
                const float g0 = u0 * __builtin_amdgcn_rcpf(1.f + __builtin_amdgcn_exp2f(z0));
                const float g1 = u1 * __builtin_amdgcn_rcpf(1.f + __builtin_amdgcn_exp2f(z1));
                y = dot2(pk_bf16(g0, g1), w2, y);
            }
            y = bfr(bfr(wave_sum(y)) + bf_lo(b2r));
            const float gd = bfr(y / (1.f + __expf(-y)));
            if ((lane & 15) == d) gate = gd;
        }
        {
            u32 ref = 0, have = 0, per = (u32)__builtin_amdgcn_s_memrealtime() >> PSHIFT;
            for (;;) {
                const u32 ld = __builtin_amdgcn_readfirstlane(*(volatile LAS u32*)(L + CT_LANDED));
                if (ld >= 2 * TPH - CSTOP || budget == 0) break;
                --budget;
                const u32 now = (u32)__builtin_amdgcn_s_memrealtime();
                const u32 lag = now * 10u - ld * TAU;
                if ((now >> PSHIFT) != per) {
                    per = now >> PSHIFT;
                    GAS char* pw = ws + WS_PROG + (per & 63) * (8 * PSTRIDE), *pr = ws + WS_PROG + ((per - 1) & 63) * (8 * PSTRIDE);
                    if (b == 0 && lane == 0) __hip_atomic_store((GAS u64*)(pw + (h & 7) * PSTRIDE + (h >> 3) * 8), ((u64)(tag0 + per) << 32) | lag, RLX_AGENT);
                    const u64 e = __hip_atomic_load((GAS u64*)(pr + (lane & 7) * PSTRIDE + ((lane >> 3) & 3) * 8), RLX_AGENT);
                    const bool ok = (u32)(e >> 32) == tag0 + per - 1;
                    const int n = __builtin_popcountll(__ballot(ok));
                    const float d = wave_sum(ok ? (float)(int)((u32)e - lag) : 0.f);
                    if (n >= 48) { ref = lag + __builtin_amdgcn_readfirstlane((int)(d / (float)n)); have = 1; }
                }
                if (have) {
                    const float dev = (float)(int)(lag - ref);
                    const float wf = fminf(fmaxf((float)WAITN + __builtin_rintf(dev * (1.f / CGAIN)), (float)WMIN), (float)WMAX);
                    *(volatile LAS u32*)(L + CT_WIN) = (u32)wf;
                }
                __builtin_amdgcn_s_sleep(32);
            }
        }
        asm volatile("s_barrier" ::: "memory");
        __builtin_amdgcn_s_sleep(28);
        CFENCE();
        __builtin_amdgcn_s_setprio(3);
        const u32 tag = __builtin_amdgcn_readfirstlane(*(volatile LAS u32*)(L + CT_TAG));
        GAS u64* in = (GAS u64*)(ws + WS_INBOX + (size_t)wg * 4096) + lane;
        u64 v[8];
#pragma unroll
        for (int k = 0; k < 8; ++k) v[k] = 0;
        u32 pend = 0xffu;
        for (;;) {
#pragma unroll
            for (int k = 0; k < 8; ++k)
                if (pend & (1u << k)) v[k] = __hip_atomic_load(in + k * 64, RLX_AGENT);
#pragma unroll
            for (int k = 0; k < 8; ++k)
                if (__all((u32)(v[k] >> 32) == tag)) pend &= ~(1u << k);
            if (pend == 0 || budget == 0) break;
            --budget;
        }
        float s = 0.f;
#pragma unroll
        for (int k = 0; k < 8; ++k) s += __uint_as_float((u32)v[k]);
        s = fold16(s, s);
        s = fold32(s, s);
        const float res = bf_lo(xo) + bfr(bfr(s) * gate);
        if (lane < 16) out[b * DM + 16 * h + lane] = (u16)pk_bf16(res, res);
        if (h == 0 && lane == 0) __hip_atomic_store((GAS u32*)(ws + WS_EPOCH + b * 128), tag, RLX_AGENT);
    } else {
        __builtin_amdgcn_s_setprio(2);
        const int ci = wave - 1, r = lane >> 4, c = lane & 15;
        v4u xr = *(const GAS v4u*)(x + b * (DM * 2) + lane * 16);
        u32 tag = __hip_atomic_load((GAS u32*)(ws + WS_EPOCH + b * 128), RLX_AGENT);
        v4u w[32];
        const int rot = b;
        {
            const __amdgpu_buffer_rsrc_t rs = __builtin_amdgcn_make_buffer_rsrc((void*)wq_, 0, DM * NH * DH * 2, 0x00020000);
            const u32 vo = 32 * r * (NH * DH * 2) + c * 16, so = 128 * ci * (NH * DH * 2) + h * (DH * 2);
#pragma unroll
            for (int i = 0; i < 32; ++i)
                w[i] = __builtin_amdgcn_raw_buffer_load_b128(rs, vo, so + ((i + 4 * rot) & 31) * (NH * DH * 2), AUX_NT);
        }
        __builtin_amdgcn_sched_barrier(0);
        asm volatile("s_barrier" ::: "memory");
        asm volatile("" : "+v"(xr), "+v"(tag) :: "memory");
        tag += TAG_STEP;
        LAS char* priv = L + PV_OFF + ci * 1024;
        {
            float xv[8], xn[8];
            unpack8(xr, xv);
            ln8(xv, xn);
            v4u o;
#pragma unroll
            for (int e = 0; e < 4; ++e) o[e] = pk_bf16(xn[2 * e], xn[2 * e + 1]);
            v2u o0 = {o.x, o.y}, o1 = {o.z, o.w};
            *(LAS v2u*)(priv + (lane >> 2) * 64 + ((4 * (lane & 3) - 2 * rot) & 15) * 4) = o0;
            *(LAS v2u*)(priv + (lane >> 2) * 64 + ((4 * (lane & 3) + 2 - 2 * rot) & 15) * 4) = o1;
        }
        if (ci == 0) *(volatile LAS u32*)(L + CT_TAG) = tag;
        pair_rows(w);
        {
            const v2f qp = gemv32(w, (const LAS v4u*)(priv + (128 * ci + 32 * r) * 2));
            *(LAS v2f*)(L + QP_OFF + ci * 512 + (8 * c + 2 * r) * 4) = qp;
        }
        CFENCE();
        cbar(L, 0, ci, budget);
        u32 q[4];
        {
            v4f s0 = *(const LAS v4f*)(L + QP_OFF + c * 32), s1 = *(const LAS v4f*)(L + QP_OFF + c * 32 + 16);
#pragma unroll
            for (int k = 1; k < 4; ++k) { s0 += *(const LAS v4f*)(L + QP_OFF + k * 512 + c * 32); s1 += *(const LAS v4f*)(L + QP_OFF + k * 512 + c * 32 + 16); }
            q[0] = pk_bf16(s0.x, s0.y); q[1] = pk_bf16(s0.z, s0.w); q[2] = pk_bf16(s1.x, s1.y); q[3] = pk_bf16(s1.z, s1.w);
        }
        tag = *(volatile LAS u32*)(L + CT_TAG);
        volatile LAS u32* freew = (volatile LAS u32*)(L + CT_FREE + ci * 256 + lane * 4);
        LAS char* myscores = L + SC_OFF + ci * 4096;
        u32 landed = 0;
        float mx = -INFINITY;
        const bool lo8 = c < 8, lo4 = (c & 4) == 0;
        const __amdgpu_buffer_rsrc_t wors = __builtin_amdgcn_make_buffer_rsrc((void*)wo_, 0, DM * NH * DH * 2, 0x00020000);
        const u32 wovo = (h * DH + 32 * r) * (DM * 2) + (128 * ci + 8 * c) * 2;
#pragma unroll
        for (int seg = 0; seg < 8; ++seg) {
#pragma unroll
            for (int i = 4 * seg; i < 4 * seg + 4; ++i) {
                const u32 vo = wovo + (i & 3) * (DM * 2), so = ((seg + rot) & 7) * (4 * DM * 2);
                if (seg) w[i] = __builtin_amdgcn_raw_buffer_load_b128(wors, vo, so, 0);
                else w[i] = __builtin_amdgcn_raw_buffer_load_b128(wors, vo, so, AUX_NT);
            }
            if (seg == 0) {
                __builtin_amdgcn_sched_barrier(0);
#pragma unroll
                for (int i = 0; i < 4; ++i) w[28 + i] = __builtin_amdgcn_raw_buffer_load_b128(wors, wovo + i * (DM * 2), rot * (4 * DM * 2), AUX_SC0);
            }
            if (seg == 1) asm volatile("" :: "v"(w[28]), "v"(w[29]), "v"(w[30]), "v"(w[31]));
            __builtin_amdgcn_sched_barrier(0);
#pragma unroll 1
            for (int it = seg * (ITERS / 8); it < (seg + 1) * (ITERS / 8); ++it) {
                const u32 tile = 4 * it + ci;
                wait_landed(L, tile, landed, budget);
                const LAS v4u* tp = (const LAS v4u*)(L + RING_OFF + (tile & (NSLOT - 1)) * TILE + lane * 16);
                v4u k0 = tp[0], k1 = tp[64], k2 = tp[128], k3 = tp[192];
                LGKM0();
                *freew = it + 1;
                asm volatile("" : "+v"(k0), "+v"(k1), "+v"(k2), "+v"(k3) :: "memory");
                float a0 = 0.f, a1 = 0.f, a2 = 0.f, a3 = 0.f;
#pragma unroll
                for (int e = 0; e < 4; ++e) { a0 = dot2(k0[e], q[e], a0); a1 = dot2(k1[e], q[e], a1); a2 = dot2(k2[e], q[e], a2); a3 = dot2(k3[e], q[e], a3); }
                const float b0 = (lo8 ? a0 : a2) + dpp<XOR8>(lo8 ? a2 : a0);
                const float b1 = (lo8 ? a1 : a3) + dpp<XOR8>(lo8 ? a3 : a1);
                float s = (lo4 ? b0 : b1) + dpp<XOR7>(lo4 ? b1 : b0);
                s += dpp<XOR2>(s);
                s += dpp<XOR1>(s);
                const float sc = bfr(s) * 0.088388347648318440f;
                const u32 sb = pk_bf16(sc, sc);
                mx = fmaxf(mx, bf_lo(sb));
                *(LAS u16*)(myscores + it * 32 + r * 8 + (c >> 2) * 2) = (u16)sb;
            }
        }
        octet_rows(w);
        PIN16(w, 0); PIN16(w, 16);
        mx = wave_max(mx);
        *(volatile LAS float*)(L + CT_MAXV + ci * 4) = mx;
        cbar(L, 1, ci, budget);
        {
            const v4f mv = *(volatile LAS v4f*)(L + CT_MAXV);
            const float gm = fmaxf(fmaxf(mv.x, mv.y), fmaxf(mv.z, mv.w));
            LAS v4u* sp = (LAS v4u*)(myscores + lane * 16);
            float ex[32];
            float sum = 0.f;
#pragma unroll
            for (int j = 0; j < 4; ++j) {
                const v4u u = sp[j * 64];
#pragma unroll
                for (int e = 0; e < 4; ++e) {
                    const float e0 = __builtin_amdgcn_exp2f((bf_lo(u[e]) - gm) * 1.4426950408889634f);
                    const float e1 = __builtin_amdgcn_exp2f((bf_hi(u[e]) - gm) * 1.4426950408889634f);
                    ex[8 * j + 2 * e] = e0; ex[8 * j + 2 * e + 1] = e1;
                    sum += e0 + e1;
                }
            }
            sum = wave_sum(sum);
            *(volatile LAS float*)(L + CT_SUMV + ci * 4) = sum;
            cbar(L, 2, ci, budget);
            const v4f sv = *(volatile LAS v4f*)(L + CT_SUMV);
            const float inv = 1.f / ((sv.x + sv.y) + (sv.z + sv.w));
#pragma unroll
            for (int j = 0; j < 4; ++j) {
                v4u u;
#pragma unroll
                for (int e = 0; e < 4; ++e) u[e] = pk_bf16(ex[8 * j + 2 * e] * inv, ex[8 * j + 2 * e + 1] * inv);
                sp[j * 64] = u;
            }
        }
        CFENCE();
        float acc[8];
#pragma unroll
        for (int e = 0; e < 8; ++e) acc[e] = 0.f;
        u32 opw = OP_OFF + ci * 512 + (8 * c + 2 * r) * 4, opr = OP_OFF + (16 * r + ((c + 2 * rot) & 15)) * 8;
        GAS char* g = ws + WS_INBOX + ((size_t)((b * 32 + 8 * ci + (c >> 1)) * 32 + (h & 7) * 4 + (h >> 3))) * 128 + (8 * (c & 1) + 2 * r) * 8;
        asm volatile("" : "+v"(opw), "+v"(opr), "+v"(g));
#pragma unroll 1
        for (int it = 0; it < ITERS; ++it) {
            const u32 tile = TPH + 4 * it + ci;
            const v2u pp = *(const LAS v2u*)(myscores + it * 32 + r * 8);
            wait_landed(L, tile, landed, budget);
            const LAS v4u* tp = (const LAS v4u*)(L + RING_OFF + (tile & (NSLOT - 1)) * TILE + lane * 16);
            v4u v0 = tp[0], v1 = tp[64], v2 = tp[128], v3 = tp[192];
            LGKM0();
            *freew = ITERS + it + 1;
            asm volatile("" : "+v"(v0), "+v"(v1), "+v"(v2), "+v"(v3) :: "memory");
#pragma unroll
            for (int e = 0; e < 4; ++e) {
                acc[2 * e] = dot2(__builtin_amdgcn_perm(v1[e], v0[e], 0x05040100u), pp.x, acc[2 * e]);
                acc[2 * e + 1] = dot2(__builtin_amdgcn_perm(v1[e], v0[e], 0x07060302u), pp.x, acc[2 * e + 1]);
            }
#pragma unroll
            for (int e = 0; e < 4; ++e) {
                acc[2 * e] = dot2(__builtin_amdgcn_perm(v3[e], v2[e], 0x05040100u), pp.y, acc[2 * e]);
                acc[2 * e + 1] = dot2(__builtin_amdgcn_perm(v3[e], v2[e], 0x07060302u), pp.y, acc[2 * e + 1]);
            }
        }
        *(LAS v2f*)(size_t)opw = fold_rows(acc);
        asm volatile("s_waitcnt lgkmcnt(0)\n\ts_barrier" ::: "memory");
        const LAS char* op = (const LAS char*)(size_t)opr;
        const v2f o0 = *(const LAS v2f*)op, o1 = *(const LAS v2f*)(op + 512), o2 = *(const LAS v2f*)(op + 1024), o3 = *(const LAS v2f*)(op + 1536);
        const v2f o = (o0 + o1) + (o2 + o3);
        const v2f sp = gemv32_mfma(w, pk_bf16(o.x, o.y), r & 1, r & 2);
        store16_agent(g, (v4u){__float_as_uint(sp.x), tag, __float_as_uint(sp.y), tag});
    }
}

extern "C" void kernel_launch(void* const* d_in, const int* in_sizes, int n_in,
                              void* d_out, int out_size, void* d_ws, size_t ws_size,
                              hipStream_t stream) {
    (void)in_sizes; (void)n_in; (void)out_size; (void)ws_size;
    fused_decode_kernel<<<NB * NH, 384, 0, stream>>>(
        (const char*)d_in[1], (const char*)d_in[3], (const char*)d_in[0], (char*)d_ws, (const char*)d_in[2], (const char*)d_in[4],
        (const char*)d_in[6], (const char*)d_in[7], (const char*)d_in[8], (const char*)d_in[9], (u16*)d_out);
}
```
